```python
import torch

def _softmax(x, axis=-1):
    return torch.softmax(x, dim=axis)
def _gelu(x):
    return torch.nn.functional.gelu(x, approximate="tanh")
def _silu(x):
    return torch.nn.functional.silu(x)
def _sigmoid(x):
    return torch.sigmoid(x)
def _layernorm(x, axis=-1, eps=1e-5):
    m = x.mean(dim=axis, keepdim=True)
    v = x.var(dim=axis, keepdim=True, unbiased=False)
    return (x - m) / torch.sqrt(v + eps)
def _scatter_add(tgt, idx, src, axis=0):
    return tgt.clone().index_add_(axis, idx, src)
def _segment_sum(src, seg, n):
    out = torch.zeros((n,) + src.shape[1:], dtype=src.dtype)
    return out.index_add_(0, seg, src)
def _pad(x, pw):
    flat = tuple(v for pair in reversed(pw) for v in pair)
    return torch.nn.functional.pad(x, flat)

kernel_name = 'recurrent_scan__linear_kernel_hedgehog__swiglu_ffn__prompt'

def setup_inputs():

    _w = (lambda a: (lambda t: t.to(torch.bfloat16) if t.is_floating_point() else t)(torch.as_tensor(a)))
    def _A_setup():
        B, S, D, H = 1, 2048, 576, 32
        Dh = D // H
        return {
            "x": _w((lambda *s: torch.randn(*s, dtype=torch.bfloat16) * 0.05)(B, S, D)),
            "wqkv": _w((lambda *s: torch.randn(*s, dtype=torch.bfloat16) * 0.2)(D, 3 * D)),
            "w_phi": _w((lambda *s: torch.randn(*s, dtype=torch.bfloat16) * 0.2)(H, Dh, Dh) * (Dh ** -0.5)),
            "wo": _w((lambda *s: torch.randn(*s, dtype=torch.bfloat16) * 0.05)(D, D)),
        }
    _A_in = _A_setup()

    def _B_setup():
        B, S, D, F = 1, 2048, 576, 4608
        return {
            "x": _w((lambda *s: torch.randn(*s, dtype=torch.bfloat16) * 0.05)(B, S, D)),
            "w_gate": _w((lambda *s: torch.randn(*s, dtype=torch.bfloat16) * 0.05)(D, F)),
            "w_up": _w((lambda *s: torch.randn(*s, dtype=torch.bfloat16) * 0.05)(D, F)),
            "w_down": _w((lambda *s: torch.randn(*s, dtype=torch.bfloat16) * 0.05)(F, D)),
        }
    _B_in = _B_setup()

    return {
        "A_x": _A_in["x"],
        "A_wqkv": _A_in["wqkv"],
        "A_w_phi": _A_in["w_phi"],
        "A_wo": _A_in["wo"],
        "B_x": _B_in["x"],
        "B_w_gate": _B_in["w_gate"],
        "B_w_up": _B_in["w_up"],
        "B_w_down": _B_in["w_down"],
    }

def reference(A_x, A_wqkv, A_w_phi, A_wo, B_x, B_w_gate, B_w_up, B_w_down):
    def _A_ref(x, wqkv, w_phi, wo):
        B, S, D = x.shape
        H = 32; Dh = D // H
        qkv = (lambda x, *d: x.permute(*d))((x @ wqkv).reshape(B, S, 3, H, Dh), 2, 0, 3, 1, 4)
        q, k, v = qkv[0], qkv[1], qkv[2]
        pq = torch.einsum("bhsd,hdf->bhsf", q, w_phi)
        pk = torch.einsum("bhsd,hdf->bhsf", k, w_phi)
        phi_q = torch.concatenate([_softmax(pq, axis=-1), _softmax(-pq, axis=-1)], axis=-1)
        phi_k = torch.concatenate([_softmax(pk, axis=-1), _softmax(-pk, axis=-1)], axis=-1)
        kv = torch.einsum("bhsf,bhsd->bhfd", phi_k, v)
        z  = torch.einsum("bhsf->bhf", phi_k)
        num = torch.einsum("bhqf,bhfd->bhqd", phi_q, kv)
        den = torch.einsum("bhqf,bhf->bhq", phi_q, z)[..., None] + 1e-6
        o = num / den
        return (lambda x, *d: x.permute(*d))(o, 0, 2, 1, 3).reshape(B, S, D) @ wo

    def _B_ref(x, w_gate, w_up, w_down):
        g = _silu(x @ w_gate)
        u = x @ w_up
        return (g * u) @ w_down

    x = A_x
    xn = _layernorm(x, axis=-1)
    s = _A_ref(xn, A_wqkv, A_w_phi, A_wo)
    g = _silu(_B_ref(xn, B_w_gate, B_w_up, B_w_down))
    return x + s * g
```

```cpp
#include <hip/hip_runtime.h>
#include <hip/hip_bf16.h>
#include <stdint.h>

typedef __attribute__((ext_vector_type(8))) short s16x8;
typedef __attribute__((ext_vector_type(4))) short s16x4;
typedef __attribute__((ext_vector_type(8))) __bf16 bf16x8;
typedef __attribute__((ext_vector_type(2))) __bf16 bf16x2;
typedef __attribute__((ext_vector_type(16))) float f32x16;
typedef __attribute__((ext_vector_type(4))) float f32x4;
typedef __attribute__((ext_vector_type(2))) float f32x2;
typedef __attribute__((ext_vector_type(4))) unsigned u32x4;
typedef __attribute__((ext_vector_type(2))) unsigned u32x2;
typedef __attribute__((ext_vector_type(4))) int i32x4;
#define LAS __attribute__((address_space(3)))
#define GAS __attribute__((address_space(1)))
#define DEV __device__ __forceinline__

template <int I> struct IC { static constexpr int v = I; };
template <int B, int E, class Fn> DEV void sfor(Fn&& fn) {
  if constexpr (B < E) { fn(IC<B>{}); sfor<B + 1, E>(fn); }
}

constexpr int S_ = 2048, D_ = 576, F_ = 4608;
constexpr size_t WS_XN = 0;
constexpr size_t WS_WOT = WS_XN + 64 * 36 * 1024;
constexpr size_t WS_WPH = WS_WOT + 36 * 18 * 1024;
constexpr size_t WS_KVP = WS_WPH + 32 * 2 * 1024;
constexpr size_t WS_O = WS_KVP + 32 * 16 * 3840;
constexpr size_t WS_CNT = WS_O + 2048 * 576 * 2;
constexpr size_t WS_YP = WS_CNT + 16 * 256;
constexpr size_t YP_BYTES = (size_t)16 * 64 * 36 * 1024;

DEV unsigned cvtpk(float lo, float hi) {
  f32x2 v = {lo, hi};
  return __builtin_bit_cast(unsigned, __builtin_convertvector(v, bf16x2));
}
DEV float bf_lo(unsigned u) { return __uint_as_float(u << 16); }
DEV float bf_hi(unsigned u) { return __uint_as_float(u & 0xffff0000u); }
DEV f32x16 mfma32(u32x4 a, u32x4 b, f32x16 c) {
  return __builtin_amdgcn_mfma_f32_32x32x16_bf16(__builtin_bit_cast(bf16x8, a), __builtin_bit_cast(bf16x8, b), c, 0, 0, 0);
}
DEV f32x4 mfma16(u32x4 a, u32x4 b, f32x4 c) {
  return __builtin_amdgcn_mfma_f32_16x16x32_bf16(__builtin_bit_cast(bf16x8, a), __builtin_bit_cast(bf16x8, b), c, 0, 0, 0);
}
DEV u32x2 trd(unsigned addr) {
  return __builtin_bit_cast(u32x2, __builtin_amdgcn_ds_read_tr16_b64_v4i16((LAS s16x4*)(uintptr_t)addr));
}
DEV u32x4 trfrag(unsigned a0, unsigned a1) {
  u32x2 lo = trd(a0), hi = trd(a1);
  u32x4 r = {lo[0], lo[1], hi[0], hi[1]};
  return r;
}
DEV u32x4 lds_rd128(unsigned a) { return *(LAS u32x4*)(uintptr_t)a; }
DEV u32x2 lds_rd64(unsigned a) { return *(LAS u32x2*)(uintptr_t)a; }
DEV void lds_wr128(unsigned a, u32x4 v) { *(LAS u32x4*)(uintptr_t)a = v; }
DEV void lds_wr64(unsigned a, u32x2 v) { *(LAS u32x2*)(uintptr_t)a = v; }
DEV i32x4 mkdesc(const void* p, unsigned bytes) {
  uint64_t a = (uint64_t)p;
  i32x4 d = {(int)__builtin_amdgcn_readfirstlane((unsigned)a), (int)__builtin_amdgcn_readfirstlane((unsigned)(a >> 32) & 0xffffu),
             (int)bytes, 0x00020000};
  return d;
}
DEV void dma16(unsigned lds, unsigned voff, i32x4 desc, unsigned soff) {
  asm volatile("s_mov_b32 m0, %0\n\ts_nop 0\n\tbuffer_load_dwordx4 %1, %2, %3 offen lds"
               :: "s"(lds), "v"(voff), "s"(desc), "s"(soff) : "memory");
}
#define WAITV(n) asm volatile("s_waitcnt vmcnt(" #n ")" ::: "memory")
#define SCHED() __builtin_amdgcn_sched_barrier(0)
DEV unsigned opq(unsigned v) { asm volatile("" : "+v"(v)); return v; }
#define WAITL0() asm volatile("s_waitcnt lgkmcnt(0)" ::: "memory")
#define BAR() do { asm volatile("" ::: "memory"); __builtin_amdgcn_s_barrier(); asm volatile("" ::: "memory"); } while (0)
DEV float xhalf_max(float x) {
  auto t = __builtin_amdgcn_permlane32_swap(__float_as_uint(x), __float_as_uint(x), false, false);
  return fmaxf(__uint_as_float(t[0]), __uint_as_float(t[1]));
}
DEV float xhalf_sum(float x) {
  auto t = __builtin_amdgcn_permlane32_swap(__float_as_uint(x), __float_as_uint(x), false, false);
  return __uint_as_float(t[0]) + __uint_as_float(t[1]);
}
constexpr float LOG2E = 1.4426950408889634f;
DEV float silu_f(float g) { return g * __builtin_amdgcn_rcpf(1.0f + __builtin_amdgcn_exp2f(-LOG2E * g)); }

DEV void hedgehog(const float (&pv)[10], int h, u32x4& pos, u32x4& neg, u32x2& ext) {
  float mx = pv[0], mn = pv[0];
#pragma unroll
  for (int r = 1; r < 8; ++r) { mx = fmaxf(mx, pv[r]); mn = fminf(mn, pv[r]); }
  if (h == 0) { mx = fmaxf(mx, fmaxf(pv[8], pv[9])); mn = fminf(mn, fminf(pv[8], pv[9])); }
  mx = xhalf_max(mx); mn = -xhalf_max(-mn);
  float ep[10], en[10], sp = 0.f, sn = 0.f;
#pragma unroll
  for (int r = 0; r < 10; ++r) {
    ep[r] = __builtin_amdgcn_exp2f((pv[r] - mx) * LOG2E);
    en[r] = __builtin_amdgcn_exp2f((mn - pv[r]) * LOG2E);
    if (r >= 8) { ep[r] = h ? 0.f : ep[r]; en[r] = h ? 0.f : en[r]; }
    sp += ep[r]; sn += en[r];
  }
  float ip = __builtin_amdgcn_rcpf(xhalf_sum(sp)), in = __builtin_amdgcn_rcpf(xhalf_sum(sn));
#pragma unroll
  for (int i = 0; i < 4; ++i) {
    pos[i] = cvtpk(ep[2 * i] * ip, ep[2 * i + 1] * ip);
    neg[i] = cvtpk(en[2 * i] * in, en[2 * i + 1] * in);
  }
  ext[0] = cvtpk(ep[8] * ip, ep[9] * ip);
  ext[1] = cvtpk(en[8] * in, en[9] * in);
}

__global__ __launch_bounds__(256) void k_prep(const unsigned short* __restrict__ x, const unsigned short* __restrict__ wo,
                                              const unsigned short* __restrict__ wphi, char* __restrict__ ws) {
  const int b = blockIdx.x, w = threadIdx.x >> 6, l = threadIdx.x & 63;
  if (w < 2) {
    const int tok = 8 * b + 4 * w + (l >> 4), i = l & 15;
    u32x2 v[9];
#pragma unroll
    for (int u = 0; u < 9; ++u) v[u] = *(const u32x2*)(x + (size_t)tok * D_ + 4 * (i + 16 * u));
    float f[36], sum = 0.f;
#pragma unroll
    for (int u = 0; u < 9; ++u) {
      f[4 * u] = bf_lo(v[u][0]); f[4 * u + 1] = bf_hi(v[u][0]); f[4 * u + 2] = bf_lo(v[u][1]); f[4 * u + 3] = bf_hi(v[u][1]);
      sum += (f[4 * u] + f[4 * u + 1]) + (f[4 * u + 2] + f[4 * u + 3]);
    }
#pragma unroll
    for (int m = 1; m < 16; m <<= 1) sum += __shfl_xor(sum, m);
    const float mean = sum * (1.0f / D_);
    float sq = 0.f;
#pragma unroll
    for (int e = 0; e < 36; ++e) { f[e] -= mean; sq += f[e] * f[e]; }
#pragma unroll
    for (int m = 1; m < 16; m <<= 1) sq += __shfl_xor(sq, m);
    const float rs = 1.0f / sqrtf(sq * (1.0f / D_) + 1e-5f);
    char* dst = ws + WS_XN + (size_t)(tok >> 5) * 36864 + (tok & 31) * 16;
#pragma unroll
    for (int u = 0; u < 9; ++u) {
      const int c8 = i + 16 * u, c = c8 >> 1;
      u32x2 o = {cvtpk(f[4 * u] * rs, f[4 * u + 1] * rs), cvtpk(f[4 * u + 2] * rs, f[4 * u + 3] * rs)};
      *(u32x2*)(dst + (c >> 1) * 1024 + (c & 1) * 512 + (c8 & 1) * 8) = o;
    }
  } else {
    for (int fi = 2 * b + (w - 2); fi < 648 + 64; fi += 512) {
      unsigned short e[8];
      char* dst;
      if (fi < 648) {
        const int hb = fi / 18, s = fi % 18, i = l & 15, g = l >> 4, gi = i >> 2;
        const int d = 16 * hb + 8 * (gi & 1) + 4 * (gi >> 1) + (i & 3);
#pragma unroll
        for (int jj = 0; jj < 8; ++jj) e[jj] = wo[(size_t)(32 * s + 8 * g + jj) * D_ + d];
        dst = ws + WS_WOT + (size_t)fi * 1024;
      } else {
        const int q = fi - 648, head = q >> 1, s = q & 1, f = l & 31, h = l >> 5;
#pragma unroll
        for (int jj = 0; jj < 8; ++jj) {
          const int d = 16 * s + 8 * (jj >> 2) + 4 * h + (jj & 3) - 2 * (head & 1);
          const bool ok = f < 18 && d >= 0 && d < 18;
          const unsigned short t = wphi[head * 324 + (ok ? d * 18 + f : 0)];
          e[jj] = ok ? t : (unsigned short)0;
        }
        dst = ws + WS_WPH + (size_t)q * 1024;
      }
      u32x4 o = {e[0] | ((unsigned)e[1] << 16), e[2] | ((unsigned)e[3] << 16), e[4] | ((unsigned)e[5] << 16), e[6] | ((unsigned)e[7] << 16)};
      *(u32x4*)(dst + l * 16) = o;
    }
  }
  if (b == 0 && threadIdx.x < 16) *(unsigned*)(ws + WS_CNT + threadIdx.x * 256) = 0u;
}

constexpr unsigned SLOT = 24576, HALF = 3 * SLOT, SPARE = 6 * SLOT, LDS_MAIN = 163840;
#define YAUX 16

__global__ __launch_bounds__(512, 2) void k_main(const unsigned short* __restrict__ wqkv, const unsigned short* __restrict__ wg,
                                                 const unsigned short* __restrict__ wu, const unsigned short* __restrict__ wd,
                                                 char* __restrict__ ws) {
  __shared__ __attribute__((aligned(1024))) char lds[LDS_MAIN];
  const unsigned L0 = (unsigned)(uintptr_t)(LAS char*)lds;
  const int b = blockIdx.x, tid = threadIdx.x;
  const int j = 2 * (b & 7) + ((b >> 3) & 1), t = b >> 4;
  const int w = __builtin_amdgcn_readfirstlane(tid >> 6), l = tid & 63;
  const int tg4 = w >> 1, p = w & 1, h = l >> 5, g16 = (l >> 4) & 1, q4 = (l >> 2) & 3, pp = l & 3;
  const int tg = 4 * t + tg4;

  const i32x4 dQ = mkdesc(wqkv, D_ * 3 * D_ * 2), dA = mkdesc(p ? wu : wg, D_ * F_ * 2), dB = mkdesc(wd, D_ * F_ * 2);
  unsigned vq[3];
#pragma unroll
  for (int m = 0; m < 3; ++m) {
    int c = 64 * (w + 8 * m) + l; c = c < 1440 ? c : 1439;
    const int k16 = c / 240, r = c % 240, seg = r / 80, pos = (r % 80) / 5, ch = r % 5;
    const int row = 16 * k16 + 8 * ((pos >> 1) & 1) + 4 * (pos & 1) + (pos >> 2);
    vq[m] = row * 3456 + seg * 1152 + ((72 * j) & ~15) + 16 * ch;
  }
  const unsigned vA = (l >> 2) * 9216 + ((((l >> 1) & 1) ^ p) * 32) + (l & 1) * 16;
  const unsigned vB = (8 * p + 4 * h + q4) * 1152 + 64 * g16 + 16 * pp;
  auto issueQ = [&](int g) {
#pragma unroll
    for (int m = 0; m < 3; ++m) dma16(L0 + g * SLOT + (w + 8 * m) * 1024, vq[m], dQ, g * 96 * 3456);
  };
  auto issueA = [&](int u, int i, unsigned dst) {
#pragma unroll
    for (int m = 0; m < 3; ++m)
      dma16(dst + (w + 8 * m) * 1024, vA, dA, (192 * i + 16 * (tg4 + 4 * m)) * 9216 + (288 * j + 32 * u) * 2);
  };
  auto issueB = [&](int gb, unsigned dst) {
#pragma unroll
    for (int m = 0; m < 3; ++m) {
      int it = 12 * gb + tg4 + 4 * m; it = it < 162 ? it : 161;
      const int dp = (it * 3641) >> 16, ks = it - 18 * dp;
      dma16(dst + (w + 8 * m) * 1024, vB, dB, (288 * j + 16 * ks) * 1152 + 128 * dp);
    }
  };

  issueQ(0); issueQ(1);
  u32x4 X[36], wp0, wp1;
  {
    const u32x4* xs = (const u32x4*)(ws + WS_XN + (size_t)tg * 36864) + l;
#pragma unroll
    for (int s = 0; s < 36; ++s) X[s] = xs[s * 64];
    const u32x4* wsrc = (const u32x4*)(ws + WS_WPH + (size_t)(2 * j + p) * 2048) + l;
    wp0 = wsrc[0]; wp1 = wsrc[64];
#pragma unroll
    for (int s = 0; s < 36; ++s) asm volatile("" : "+v"(X[s]));
    asm volatile("" : "+v"(wp0), "+v"(wp1));
  }
  issueQ(2); issueQ(3); issueQ(4);

  f32x16 aq = {}, ak = {}, av = {};
  {
    const unsigned rq = L0 + (4 * q4 + 2 * h) * 80 + ((72 * j) & 15) + 32 * p + 32 * g16 + 8 * pp;
#define QADDR(n) (rb[((n) / 18) & 1] + (((n) % 18) / 3) * 3840 + ((n) % 3) * 1280)
    u32x4 a[3];
    unsigned rb[2] = {rq, rq};
    sfor<0, 108>([&](auto nn) {
      constexpr int n = decltype(nn)::v;
      if (n % 18 == 0) {
        constexpr int g = n / 18;
        WAITV(9); BAR();
        rb[(g + 1) & 1] = opq(rq + (g + 1) * SLOT);
        if (g == 0) issueQ(5); else issueA((g - 1) / 3, (g - 1) % 3, L0 + (g - 1) * SLOT);
        if (n == 0) {
#pragma unroll
          for (int e = 0; e < 3; ++e) a[e] = trfrag(QADDR(e), QADDR(e) + 80);
        }
      }
      const int s = n / 3;
      if (n % 3 == 0) aq = mfma32(a[0], X[s], aq);
      else if (n % 3 == 1) ak = mfma32(a[1], X[s], ak);
      else av = mfma32(X[s], a[2], av);
      if (n + 3 < 108) a[n % 3] = trfrag(QADDR(n + 3), QADDR(n + 3) + 80);
      if (n == 107) asm volatile("" : "+v"(aq), "+v"(ak), "+v"(av));
      SCHED();
    });
  }
  unsigned pqk[5];
  {
    auto phi = [&](const f32x16& c) {
      u32x4 b0 = {cvtpk(c[0], c[1]), cvtpk(c[2], c[3]), cvtpk(c[4], c[5]), cvtpk(c[6], c[7])};
      u32x4 b1 = {cvtpk(c[8], c[9]), cvtpk(c[10], c[11]), 0u, 0u};
      b0[0] = (p && !h) ? 0u : b0[0];
      b1[0] = h ? 0u : b1[0];
      b1[1] = (p && !h) ? b1[1] : 0u;
      f32x16 z = {};
      z = mfma32(wp0, b0, z);
      return mfma32(wp1, b1, z);
    };
    const f32x16 pq = phi(aq), pk = phi(ak);
#pragma unroll
    for (int i = 0; i < 5; ++i) pqk[i] = cvtpk(pq[2 * i], pq[2 * i + 1]);
    float pv[10];
#pragma unroll
    for (int i = 0; i < 5; ++i) { const unsigned u = cvtpk(pk[2 * i], pk[2 * i + 1]); pv[2 * i] = bf_lo(u); pv[2 * i + 1] = bf_hi(u); }
    u32x4 pos, neg; u32x2 ext;
    hedgehog(pv, h, pos, neg, ext);
    BAR();
    const unsigned R = L0 + 5 * SLOT + w * 5120, tk = l & 31;
    lds_wr64(R + tk * 64 + 8 * h, u32x2{pos[0], pos[1]});
    lds_wr64(R + tk * 64 + 16 + 8 * h, u32x2{pos[2], pos[3]});
    lds_wr64(R + tk * 64 + 32 + 8 * h, u32x2{neg[0], neg[1]});
    lds_wr64(R + tk * 64 + 48 + 8 * h, u32x2{neg[2], neg[3]});
    lds_wr64(R + 2048 + l * 8, ext);
    asm volatile("" ::: "memory");
    f32x16 kv0 = {}, kv1 = {};
#pragma unroll
    for (int s = 0; s < 2; ++s) {
      u32x4 vb;
#pragma unroll
      for (int i = 0; i < 4; ++i) vb[i] = (tk == 20) ? 0x3f803f80u : cvtpk(av[8 * s + 2 * i], av[8 * s + 2 * i + 1]);
      const unsigned r0 = 16 * s + 4 * h + q4;
      kv0 = mfma32(trfrag(R + r0 * 64 + 32 * g16 + 8 * pp, R + (r0 + 8) * 64 + 32 * g16 + 8 * pp), vb, kv0);
      kv1 = mfma32(trfrag(R + 2048 + r0 * 8, R + 2048 + (r0 + 8) * 8), vb, kv1);
    }
    asm volatile("" ::: "memory");
#pragma unroll
    for (int qd = 0; qd < 4; ++qd)
      lds_wr128(R + qd * 1024 + l * 16, __builtin_bit_cast(u32x4, f32x4{kv0[4 * qd], kv0[4 * qd + 1], kv0[4 * qd + 2], kv0[4 * qd + 3]}));
    lds_wr128(R + 4096 + l * 16, __builtin_bit_cast(u32x4, f32x4{kv1[0], kv1[1], kv1[2], kv1[3]}));
    WAITL0(); BAR();
    if (tid < 480) {
      const int pq_ = tid / 240, rem = tid % 240, qd = rem / 48, li = rem % 48, ln = (li / 24) * 32 + li % 24;
      f32x4 sum = {};
#pragma unroll
      for (int e = 0; e < 4; ++e) sum += __builtin_bit_cast(f32x4, lds_rd128(L0 + 5 * SLOT + (2 * e + pq_) * 5120 + qd * 1024 + ln * 16));
      __builtin_amdgcn_raw_buffer_store_b128(__builtin_bit_cast(u32x4, sum),
          __builtin_amdgcn_make_buffer_rsrc(ws + WS_KVP, 0, 32 * 16 * 3840, 0x00020000),
          ((2 * j + pq_) * 16 + t) * 3840 + rem * 16, 0, 16);
    }
    WAITL0();
  }

  u32x4 H[18];
  {
    unsigned cur = L0, oth = L0 + HALF;
    const unsigned rA = g16 * 1024 + h * 512 + q4 * 64 + (p ^ g16) * 32 + pp * 8;
    u32x4 a[3], hh[9];
#pragma unroll
    for (int e = 0; e < 9; ++e) hh[e] = u32x4{0u, 0u, 0u, 0u};
#pragma unroll
    for (int e = 0; e < 3; ++e) a[e] = trfrag(cur + rA + e * 2048, cur + rA + e * 2048 + 256);
#pragma unroll 1
    for (int u = 0; u < 9; ++u) {
      const unsigned rc = opq(cur + rA), rc2 = opq(cur + rA + 2 * SLOT), rn = opq(oth + rA);
      f32x16 acc = {};
#pragma unroll
      for (int s = 0; s < 36; ++s) {
        if (s % 12 == 0) {
          const int i = s / 12;
          if (i == 1 && u == 0) WAITV(0); else WAITV(9);
          BAR();
          if (i == 1 && u == 0 && tid == 0)
            __hip_atomic_fetch_add((GAS unsigned*)(ws + WS_CNT + j * 256), 1u, __ATOMIC_RELAXED, __HIP_MEMORY_SCOPE_AGENT);
          if (i == 0) { if (u < 8) issueA(u + 1, 2, oth + 2 * SLOT); else issueB(2, oth + 2 * SLOT); }
          else if (u < 7) issueA(u + 2, i - 1, cur + (i - 1) * SLOT);
          else if (u == 7) issueB(i - 1, cur + (i - 1) * SLOT);
        }
        acc = mfma32(a[s % 3], X[s], acc);
        const int sn = s + 3;
        const unsigned ad = sn < 24 ? rc + sn * 2048 : sn < 36 ? rc2 + (sn - 24) * 2048 : rn + (sn - 36) * 2048;
        a[s % 3] = trfrag(ad, ad + 256);
        SCHED();
      }
#pragma unroll
      for (int e = 0; e < 8; ++e) hh[e] = hh[e + 1];
#pragma unroll
      for (int i = 0; i < 4; ++i) hh[8][i] = cvtpk(silu_f(acc[2 * i]) * acc[8 + 2 * i], silu_f(acc[2 * i + 1]) * acc[9 + 2 * i]);
      const unsigned tmp = cur; cur = oth; oth = tmp;
    }
    bool timeout = false;
    if (w == 0) {
      unsigned spins = 0;
      while (__hip_atomic_load((GAS unsigned*)(ws + WS_CNT + j * 256), __ATOMIC_RELAXED, __HIP_MEMORY_SCOPE_AGENT) < 16u) {
        __builtin_amdgcn_s_sleep(2);
        if (++spins > (1u << 22)) { timeout = true; break; }
      }
    }
    WAITL0(); BAR();
    u32x4 kp[16];
    if (tid < 480) {
      const int pq_ = tid / 240, rem = tid % 240;
#pragma unroll
      for (int e = 0; e < 16; ++e)
        kp[e] = __builtin_amdgcn_raw_buffer_load_b128(__builtin_amdgcn_make_buffer_rsrc(ws + WS_KVP, 0, 32 * 16 * 3840, 0x00020000),
                                                      ((2 * j + pq_) * 16 + e) * 3840 + rem * 16, 0, 16);
    }
#pragma unroll
    for (int e = 0; e < 9; ++e) lds_wr128(L0 + (tg4 * 18 + 2 * e + p) * 1024 + l * 16, hh[e]);
    if (tid < 480) {
      f32x4 sum = {};
#pragma unroll
      for (int e = 0; e < 16; ++e) sum += __builtin_bit_cast(f32x4, kp[e]);
      if (timeout) sum[0] = __uint_as_float(0x7fc00000u);
      lds_wr64(L0 + SPARE + tid * 8, u32x2{cvtpk(sum[0], sum[1]), cvtpk(sum[2], sum[3])});
    }
    WAITL0(); BAR();
#pragma unroll
    for (int s = 0; s < 18; ++s) H[s] = lds_rd128(L0 + (tg4 * 18 + s) * 1024 + l * 16);
    u32x4 k0, k1, k2;
    {
      const bool ok = (l & 31) < 24;
      const unsigned ka = L0 + SPARE + (p * 240 + h * 24 + (ok ? (l & 31) : 0)) * 8;
      const u32x2 c0 = lds_rd64(ka), c1 = lds_rd64(ka + 384), c2 = lds_rd64(ka + 768), c3 = lds_rd64(ka + 1152), c4 = lds_rd64(ka + 1536);
      k0 = u32x4{c0[0], c0[1], c1[0], c1[1]}; k1 = u32x4{c2[0], c2[1], c3[0], c3[1]};
      k2 = u32x4{h ? 0u : c4[0], h ? 0u : c4[1], 0u, 0u};
    }
    WAITL0();
    WAITV(3); BAR();
    issueB(3, L0); issueB(4, L0 + SLOT); issueB(5, L0 + 2 * SLOT);
    float pv[10];
#pragma unroll
    for (int i = 0; i < 5; ++i) { pv[2 * i] = bf_lo(pqk[i]); pv[2 * i + 1] = bf_hi(pqk[i]); }
    u32x4 pos, neg; u32x2 ext;
    hedgehog(pv, h, pos, neg, ext);
    f32x16 on = {};
    on = mfma32(k0, pos, on); on = mfma32(k1, neg, on); on = mfma32(k2, u32x4{ext[0], ext[1], 0u, 0u}, on);
    const float inv = __builtin_amdgcn_rcpf(xhalf_sum(h ? on[8] : 0.f) + 1e-6f);
    char* op = ws + WS_O + (size_t)(32 * tg + (l & 31)) * 1152 + (36 * j + 16 * p + 4 * h) * 2;
    const u32x2 o0 = {cvtpk(on[0] * inv, on[1] * inv), cvtpk(on[2] * inv, on[3] * inv)};
    const u32x2 o1 = {cvtpk(on[4] * inv, on[5] * inv), cvtpk(on[6] * inv, on[7] * inv)};
    const u32x2 o2 = {cvtpk(on[8] * inv, on[9] * inv), cvtpk(on[10] * inv, on[11] * inv)};
    if (p && !h) *(unsigned*)(op + 4) = o0[1]; else *(u32x2*)op = o0;
    *(u32x2*)(op + 16) = o1;
    if (!h) { if (p) *(u32x2*)(op + 32) = o2; else *(unsigned*)(op + 32) = o2[0]; }
  }

  {
    unsigned cur = L0 + HALF, oth = L0;
    const unsigned rB = h * 512 + p * 256 + q4 * 64 + g16 * 32 + pp * 8;
    const auto yr = __builtin_amdgcn_make_buffer_rsrc(ws + WS_YP, 0, (unsigned)YP_BYTES, 0x00020000);
    const unsigned ybase = ((j * 64 + tg) * 36 + 2 * p) * 1024;
    u32x4 a[3];
#pragma unroll
    for (int e = 0; e < 3; ++e) a[e] = trfrag(cur + rB + e * 2048, cur + rB + e * 2048 + 1024);
#pragma unroll 1
    for (int P = 0; P < 5; ++P) {
      const unsigned rc = opq(cur + rB), rc2 = opq(cur + rB + 2 * SLOT), rn = opq(oth + rB);
      f32x16 acc = {};
#pragma unroll
      for (int n = 0; n < 36; ++n) {
        if (n == 18 && P == 4) break;
        if (n % 12 == 0 && !(n == 0 && P == 0)) {
          const int i = n / 12, gb = 3 * P + i;
          if (gb < 9) WAITV(9); else if (gb < 13) WAITV(2);
          BAR();
          if (gb + 5 <= 13) issueB(gb + 5, i == 0 ? oth + 2 * SLOT : cur + (i - 1) * SLOT);
        }
        acc = mfma32(a[n % 3], H[n % 18], acc);
        const int sn = n + 3;
        const unsigned ad = sn < 24 ? rc + sn * 2048 : sn < 36 ? rc2 + (sn - 24) * 2048 : rn + (sn - 36) * 2048;
        a[n % 3] = trfrag(ad, ad + 1024);
        SCHED();
        if (n % 18 == 17) {
          const unsigned so = ybase + (2 * P + n / 18) * 4096;
#pragma unroll
          for (int e = 0; e < 2; ++e) {
            const u32x4 y = {cvtpk(acc[8 * e], acc[8 * e + 1]), cvtpk(acc[8 * e + 2], acc[8 * e + 3]),
                             cvtpk(acc[8 * e + 4], acc[8 * e + 5]), cvtpk(acc[8 * e + 6], acc[8 * e + 7])};
            __builtin_amdgcn_raw_buffer_store_b128(y, yr, l * 16 + e * 1024, so, YAUX);
          }
          acc = f32x16{};
        }
      }
      const unsigned tmp = cur; cur = oth; oth = tmp;
    }
  }
}

__global__ __launch_bounds__(576, 3) void k_final(const unsigned short* __restrict__ x, const char* __restrict__ ws,
                                                  unsigned short* __restrict__ out) {
  __shared__ __attribute__((aligned(1024))) char lds[36864];
  const unsigned L0 = (unsigned)(uintptr_t)(LAS char*)lds;
  const int b = blockIdx.x, tg = (b & 7) * 8 + (b >> 5), dq = (b >> 3) & 3;
  const int w = __builtin_amdgcn_readfirstlane(threadIdx.x >> 6), l = threadIdx.x & 63, g = l >> 4, hb = 9 * dq + w;
  const i32x4 dO = mkdesc(ws + WS_O, S_ * D_ * 2);
#pragma unroll
  for (int m = 0; m < 4; ++m) {
    const int fi = 4 * w + m, tb = fi >= 18, s = fi - 18 * tb;
    dma16(L0 + fi * 1024, (l & 15) * 1152 + g * 16, dO, (32 * tg + 16 * tb) * 1152 + 64 * s);
  }
  u32x4 wf[18];
  const u32x4* wsrc = (const u32x4*)(ws + WS_WOT + (size_t)hb * 18432) + l;
#pragma unroll
  for (int s = 0; s < 18; ++s) wf[s] = wsrc[s * 64];
  const size_t xo = (size_t)(32 * tg + (l & 15)) * D_ + 16 * hb + 8 * (g & 1) + 4 * (g >> 1);
  const u32x2 x0 = *(const u32x2*)(x + xo), x1 = *(const u32x2*)(x + xo + 16 * D_);
  u32x2 yp[16][2];
  const char* ysrc = ws + WS_YP + (size_t)(tg * 36 + hb) * 1024 + ((g >> 1) * 32 + (l & 15)) * 16 + (g & 1) * 8;
#pragma unroll
  for (int jj = 0; jj < 16; ++jj) {
    yp[jj][0] = *(const u32x2*)(ysrc + (size_t)jj * (64 * 36 * 1024));
    yp[jj][1] = *(const u32x2*)(ysrc + (size_t)jj * (64 * 36 * 1024) + 256);
  }
  WAITV(52); BAR();
  f32x4 s0 = {}, s1 = {};
#pragma unroll
  for (int s = 0; s < 18; ++s) {
    s0 = mfma16(wf[s], lds_rd128(L0 + s * 1024 + l * 16), s0);
    s1 = mfma16(wf[s], lds_rd128(L0 + (18 + s) * 1024 + l * 16), s1);
  }
  f32x4 y0 = {}, y1 = {};
#pragma unroll
  for (int jj = 0; jj < 16; ++jj) {
    y0 += f32x4{bf_lo(yp[jj][0][0]), bf_hi(yp[jj][0][0]), bf_lo(yp[jj][0][1]), bf_hi(yp[jj][0][1])};
    y1 += f32x4{bf_lo(yp[jj][1][0]), bf_hi(yp[jj][1][0]), bf_lo(yp[jj][1][1]), bf_hi(yp[jj][1][1])};
  }
  const u32x2 r0 = {cvtpk(bf_lo(x0[0]) + s0[0] * silu_f(y0[0]), bf_hi(x0[0]) + s0[1] * silu_f(y0[1])),
                    cvtpk(bf_lo(x0[1]) + s0[2] * silu_f(y0[2]), bf_hi(x0[1]) + s0[3] * silu_f(y0[3]))};
  const u32x2 r1 = {cvtpk(bf_lo(x1[0]) + s1[0] * silu_f(y1[0]), bf_hi(x1[0]) + s1[1] * silu_f(y1[1])),
                    cvtpk(bf_lo(x1[1]) + s1[2] * silu_f(y1[2]), bf_hi(x1[1]) + s1[3] * silu_f(y1[3]))};
  *(u32x2*)(out + xo) = r0;
  *(u32x2*)(out + xo + 16 * D_) = r1;
}

extern "C" void kernel_launch(void* const* d_in, const int* in_sizes, int n_in, void* d_out, int out_size, void* d_ws,
                              size_t ws_size, hipStream_t stream) {
  const unsigned short* x = (const unsigned short*)d_in[0];
  const unsigned short* wqkv = (const unsigned short*)d_in[1];
  const unsigned short* wphi = (const unsigned short*)d_in[2];
  const unsigned short* wo = (const unsigned short*)d_in[3];
  const unsigned short* wg = (const unsigned short*)d_in[5];
  const unsigned short* wu = (const unsigned short*)d_in[6];
  const unsigned short* wd = (const unsigned short*)d_in[7];
  char* ws = (char*)d_ws;
  k_prep<<<256, 256, 0, stream>>>(x, wo, wphi, ws);
  k_main<<<256, 512, 0, stream>>>(wqkv, wg, wu, wd, ws);
  k_final<<<256, 576, 0, stream>>>(x, ws, (unsigned short*)d_out);
}
```

```cpp
#include <hip/hip_runtime.h>
#include <hip/hip_bf16.h>
#include <stdint.h>

typedef __attribute__((ext_vector_type(8))) short s16x8;
typedef __attribute__((ext_vector_type(4))) short s16x4;
typedef __attribute__((ext_vector_type(8))) __bf16 bf16x8;
typedef __attribute__((ext_vector_type(2))) __bf16 bf16x2;
typedef __attribute__((ext_vector_type(16))) float f32x16;
typedef __attribute__((ext_vector_type(4))) float f32x4;
typedef __attribute__((ext_vector_type(2))) float f32x2;
typedef __attribute__((ext_vector_type(4))) unsigned u32x4;
typedef __attribute__((ext_vector_type(2))) unsigned u32x2;
typedef __attribute__((ext_vector_type(4))) int i32x4;
#define LAS __attribute__((address_space(3)))
#define GAS __attribute__((address_space(1)))
#define DEV __device__ __forceinline__

template <int I> struct IC { static constexpr int v = I; };
template <int B, int E, class Fn> DEV void sfor(Fn&& fn) {
  if constexpr (B < E) { fn(IC<B>{}); sfor<B + 1, E>(fn); }
}

constexpr int S_ = 2048, D_ = 576, F_ = 4608;
constexpr size_t WS_XN = 0;
constexpr size_t WS_WOT = WS_XN + 64 * 36 * 1024;
constexpr size_t WS_KVP = WS_WOT + 36 * 18 * 1024;
constexpr size_t WS_O = WS_KVP + 32 * 16 * 3840;
constexpr size_t WS_CNT = WS_O + 2048 * 576 * 2;
constexpr size_t WS_KVF = WS_CNT + 32 * 256;
constexpr size_t WS_QI = WS_KVF + 16 * 4096;
constexpr size_t WS_YP = WS_QI + 16 * 6 * 24576;
constexpr size_t YP_BYTES = (size_t)16 * 64 * 36 * 1024;

DEV unsigned cvtpk(float lo, float hi) {
  f32x2 v = {lo, hi};
  return __builtin_bit_cast(unsigned, __builtin_convertvector(v, bf16x2));
}
DEV float bf_lo(unsigned u) { return __uint_as_float(u << 16); }
DEV float bf_hi(unsigned u) { return __uint_as_float(u & 0xffff0000u); }
DEV f32x16 mfma32(u32x4 a, u32x4 b, f32x16 c) {
  return __builtin_amdgcn_mfma_f32_32x32x16_bf16(__builtin_bit_cast(bf16x8, a), __builtin_bit_cast(bf16x8, b), c, 0, 0, 0);
}
DEV f32x4 mfma16(u32x4 a, u32x4 b, f32x4 c) {
  return __builtin_amdgcn_mfma_f32_16x16x32_bf16(__builtin_bit_cast(bf16x8, a), __builtin_bit_cast(bf16x8, b), c, 0, 0, 0);
}
DEV u32x2 trd(unsigned addr) {
  return __builtin_bit_cast(u32x2, __builtin_amdgcn_ds_read_tr16_b64_v4i16((LAS s16x4*)(uintptr_t)addr));
}
DEV u32x4 trfrag(unsigned a0, unsigned a1) {
  u32x2 lo = trd(a0), hi = trd(a1);
  u32x4 r = {lo[0], lo[1], hi[0], hi[1]};
  return r;
}
DEV u32x4 lds_rd128(unsigned a) { return *(LAS u32x4*)(uintptr_t)a; }
DEV u32x2 lds_rd64(unsigned a) { return *(LAS u32x2*)(uintptr_t)a; }
DEV void lds_wr128(unsigned a, u32x4 v) { *(LAS u32x4*)(uintptr_t)a = v; }
DEV void lds_wr64(unsigned a, u32x2 v) { *(LAS u32x2*)(uintptr_t)a = v; }
DEV i32x4 mkdesc(const void* p, unsigned bytes) {
  uint64_t a = (uint64_t)p;
  i32x4 d = {(int)__builtin_amdgcn_readfirstlane((unsigned)a), (int)__builtin_amdgcn_readfirstlane((unsigned)(a >> 32) & 0xffffu),
             (int)bytes, 0x00020000};
  return d;
}
DEV void dma16(unsigned lds, unsigned voff, i32x4 desc, unsigned soff) {
  asm volatile("s_mov_b32 m0, %0\n\ts_nop 0\n\tbuffer_load_dwordx4 %1, %2, %3 offen lds"
               :: "s"(lds), "v"(voff), "s"(desc), "s"(soff) : "memory");
}
#define WAITV(n) asm volatile("s_waitcnt vmcnt(" #n ")" ::: "memory")
#define ST(k)
#define STR(k)
#define SCHED() __builtin_amdgcn_sched_barrier(0)
DEV unsigned opq(unsigned v) { asm volatile("" : "+v"(v)); return v; }
#define WAITL0() asm volatile("s_waitcnt lgkmcnt(0)" ::: "memory")
#define BAR() do { asm volatile("" ::: "memory"); __builtin_amdgcn_s_barrier(); asm volatile("" ::: "memory"); } while (0)
DEV float xhalf_max(float x) {
  auto t = __builtin_amdgcn_permlane32_swap(__float_as_uint(x), __float_as_uint(x), false, false);
  return fmaxf(__uint_as_float(t[0]), __uint_as_float(t[1]));
}
DEV float xhalf_sum(float x) {
  auto t = __builtin_amdgcn_permlane32_swap(__float_as_uint(x), __float_as_uint(x), false, false);
  return __uint_as_float(t[0]) + __uint_as_float(t[1]);
}
constexpr float LOG2E = 1.4426950408889634f;
DEV float silu_f(float g) { return g * __builtin_amdgcn_rcpf(1.0f + __builtin_amdgcn_exp2f(-LOG2E * g)); }

DEV void hedgehog(const float (&pv)[10], int h, u32x4& pos, u32x4& neg, u32x2& ext) {
  float mx = pv[0], mn = pv[0];
#pragma unroll
  for (int r = 1; r < 8; ++r) { mx = fmaxf(mx, pv[r]); mn = fminf(mn, pv[r]); }
  if (h == 0) { mx = fmaxf(mx, fmaxf(pv[8], pv[9])); mn = fminf(mn, fminf(pv[8], pv[9])); }
  mx = xhalf_max(mx); mn = -xhalf_max(-mn);
  float ep[10], en[10], sp = 0.f, sn = 0.f;
#pragma unroll
  for (int r = 0; r < 10; ++r) {
    ep[r] = __builtin_amdgcn_exp2f((pv[r] - mx) * LOG2E);
    en[r] = __builtin_amdgcn_exp2f((mn - pv[r]) * LOG2E);
    if (r >= 8) { ep[r] = h ? 0.f : ep[r]; en[r] = h ? 0.f : en[r]; }
    sp += ep[r]; sn += en[r];
  }
  float ip = __builtin_amdgcn_rcpf(xhalf_sum(sp)), in = __builtin_amdgcn_rcpf(xhalf_sum(sn));
#pragma unroll
  for (int i = 0; i < 4; ++i) {
    pos[i] = cvtpk(ep[2 * i] * ip, ep[2 * i + 1] * ip);
    neg[i] = cvtpk(en[2 * i] * in, en[2 * i + 1] * in);
  }
  ext[0] = cvtpk(ep[8] * ip, ep[9] * ip);
  ext[1] = cvtpk(en[8] * in, en[9] * in);
}

__global__ __launch_bounds__(512) void k_prep(const unsigned short* __restrict__ x, const unsigned short* __restrict__ wqkv,
                                              const unsigned short* __restrict__ wo,
                                              const unsigned short* __restrict__ wphi, char* __restrict__ ws) {
  const int b = blockIdx.x, w = threadIdx.x >> 6, l = threadIdx.x & 63;
  if (w < 2) {
    const int tok = 8 * b + 4 * w + (l >> 4), i = l & 15;
    u32x2 v[9];
#pragma unroll
    for (int u = 0; u < 9; ++u) v[u] = *(const u32x2*)(x + (size_t)tok * D_ + 4 * (i + 16 * u));
    float f[36], sum = 0.f;
#pragma unroll
    for (int u = 0; u < 9; ++u) {
      f[4 * u] = bf_lo(v[u][0]); f[4 * u + 1] = bf_hi(v[u][0]); f[4 * u + 2] = bf_lo(v[u][1]); f[4 * u + 3] = bf_hi(v[u][1]);
      sum += (f[4 * u] + f[4 * u + 1]) + (f[4 * u + 2] + f[4 * u + 3]);
    }
#pragma unroll
    for (int m = 1; m < 16; m <<= 1) sum += __shfl_xor(sum, m);
    const float mean = sum * (1.0f / D_);
    float sq = 0.f;
#pragma unroll
    for (int e = 0; e < 36; ++e) { f[e] -= mean; sq += f[e] * f[e]; }
#pragma unroll
    for (int m = 1; m < 16; m <<= 1) sq += __shfl_xor(sq, m);
    const float rs = 1.0f / sqrtf(sq * (1.0f / D_) + 1e-5f);
    char* dst = ws + WS_XN + (size_t)(tok >> 5) * 36864 + (tok & 31) * 16;
#pragma unroll
    for (int u = 0; u < 9; ++u) {
      const int c8 = i + 16 * u, c = c8 >> 1;
      u32x2 o = {cvtpk(f[4 * u] * rs, f[4 * u + 1] * rs), cvtpk(f[4 * u + 2] * rs, f[4 * u + 3] * rs)};
      *(u32x2*)(dst + (c >> 1) * 1024 + (c & 1) * 512 + (c8 & 1) * 8) = o;
    }
  } else if (w >= 4) {
    u32x4 v[3];
#pragma unroll
    for (int m = 0; m < 3; ++m) {
      const int e = min((int)threadIdx.x - 256 + 256 * m, 575), id = 576 * b + e, jj = id / 9216, g = (id % 9216) / 1536;
      const int c = min(id % 1536, 1439), k16 = c / 240, r = c % 240, seg = r / 80, pos = (r % 80) / 5, ch = r % 5;
      const int row = 96 * g + 16 * k16 + 8 * ((pos >> 1) & 1) + 4 * (pos & 1) + (pos >> 2);
      const int off = min(row * 3456 + seg * 1152 + ((72 * jj) & ~15) + 16 * ch, D_ * 3 * D_ * 2 - 16);
      v[m] = *(const u32x4*)((const char*)wqkv + off);
    }
#pragma unroll
    for (int m = 0; m < 3; ++m) {
      const int e = (int)threadIdx.x - 256 + 256 * m;
      const int id = 576 * b + e, g = (id % 9216) / 1536;
      if (e < 576 && !(id % 1536 >= 1440 && (g == 2 || g == 3))) *(u32x4*)(ws + WS_QI + (size_t)id * 16) = v[m];
    }
  } else {
    for (int fi = 2 * b + (w - 2); fi < 648 + 64; fi += 512) {
      unsigned short e[8];
      char* dst;
      if (fi < 648) {
        const int hb = fi / 18, s = fi % 18, i = l & 15, g = l >> 4, gi = i >> 2;
        const int d = 16 * hb + 8 * (gi & 1) + 4 * (gi >> 1) + (i & 3);
#pragma unroll
        for (int jj = 0; jj < 8; ++jj) e[jj] = wo[(size_t)(32 * s + 8 * g + jj) * D_ + d];
        dst = ws + WS_WOT + (size_t)fi * 1024;
      } else {
        const int q = fi - 648, head = q >> 1, s = q & 1, f = l & 31, h = l >> 5;
#pragma unroll
        for (int jj = 0; jj < 8; ++jj) {
          const int d = 16 * s + 8 * (jj >> 2) + 4 * h + (jj & 3) - 2 * (head & 1);
          const bool ok = f < 18 && d >= 0 && d < 18;
          const unsigned short t = wphi[head * 324 + (ok ? d * 18 + f : 0)];
          e[jj] = ok ? t : (unsigned short)0;
        }
        dst = ws + WS_QI + (size_t)((head >> 1) * 6 + 2 + (head & 1)) * 24576 + 23040;
        if (s) {
          *(u32x2*)(dst + 1024 + l * 8) = u32x2{e[0] | ((unsigned)e[1] << 16), e[2] | ((unsigned)e[3] << 16)};
          continue;
        }
      }
      u32x4 o = {e[0] | ((unsigned)e[1] << 16), e[2] | ((unsigned)e[3] << 16), e[4] | ((unsigned)e[5] << 16), e[6] | ((unsigned)e[7] << 16)};
      *(u32x4*)(dst + l * 16) = o;
    }
  }
  if (b == 0 && threadIdx.x < 32) *(unsigned*)(ws + WS_CNT + threadIdx.x * 256) = 0u;
}

constexpr unsigned SLOT = 24576, HALF = 3 * SLOT, SPARE = 6 * SLOT, LDS_MAIN = 163840;
#define JMAP (2 * (b & 7) + ((b >> 3) & 1))
#define TMAP (b >> 4)
#define DPOS 1
#define YAUX 16

__global__ __launch_bounds__(512, 2) void k_main(const unsigned short* __restrict__ wg, const unsigned short* __restrict__ wu,
                                                 const unsigned short* __restrict__ wd, char* __restrict__ ws) {
  __shared__ __attribute__((aligned(1024))) char lds[LDS_MAIN];
  const unsigned L0 = (unsigned)(uintptr_t)(LAS char*)lds;
  const int b = blockIdx.x, tid = threadIdx.x;
  const int j = JMAP, t = TMAP, jo = j & 1;
  const int w = __builtin_amdgcn_readfirstlane(tid >> 6), tg4 = w >> 1, p = w & 1;
  int l = tid & 63, h = l >> 5, g16 = (l >> 4) & 1, q4 = (l >> 2) & 3, pp = l & 3;
  const int tg = 4 * t + tg4;
  GAS unsigned* const cnt1 = (GAS unsigned*)(ws + WS_CNT + j * 256);
  GAS unsigned* const cnt2 = cnt1 + 1024;

  const i32x4 dW = mkdesc(ws, (unsigned)WS_YP), dA = mkdesc(p ? wu : wg, D_ * F_ * 2), dB = mkdesc(wd, D_ * F_ * 2);
  unsigned l16 = l * 16;
  const unsigned vA2 = (l >> 3) * 9216 + ((((l >> 1) & 3) ^ (((l >> 4) & 1) + 2 * p)) * 32) + (l & 1) * 16;
  const unsigned vA1 = (l >> 2) * 9216 + ((((l >> 1) & 1) ^ p) * 32) + (l & 1) * 16;
  const unsigned vB = (8 * p + 4 * h + q4) * 1152 + 64 * g16 + 16 * pp;
  auto pieceX = [&](int half, int m) {
    const int n = 9 * w + m, tq = w >> 1, s = 18 * half + n - 18 * tq;
    dma16(L0 + n * 1024, l16, dW, (unsigned)WS_XN + ((4 * t + tq) * 36 + s) * 1024);
  };
  auto pieceQ = [&](int g, int m) {
    dma16(L0 + (3 + g % 3) * SLOT + (w + 8 * m) * 1024, l16, dW, (unsigned)WS_QI + (j * 6 + g) * SLOT + (w + 8 * m) * 1024);
  };
  auto pieceA2 = [&](int v, int i, int m) {
    dma16(L0 + i * SLOT + (w + 8 * m) * 1024, vA2, dA,
          (96 * i + 16 * ((w >> 2) + 2 * m) + 8 * (tg4 & 1)) * 9216 + (288 * j + 32 * jo + 64 * v) * 2);
  };
  auto pieceA1 = [&](int i, int m) {
    dma16(L0 + i * SLOT + (w + 8 * m) * 1024, vA1, dA, (192 * i + 16 * (tg4 + 4 * m)) * 9216 + (288 * j + 256 * (1 - jo)) * 2);
  };
  auto pieceB = [&](int gb, unsigned dst, int m) {
    int it = 12 * gb + tg4 + 4 * m; it = it < 162 ? it : 161;
    const int dp = (it * 3641) >> 16, ks = it - 18 * dp;
    dma16(dst + (w + 8 * m) * 1024, vB, dB, (288 * j + 16 * ks) * 1152 + 128 * dp);
  };
  bool timeout = false;
  auto spin16 = [&](GAS unsigned* c, unsigned seen) {
    unsigned spins = 0;
    while (__builtin_amdgcn_readfirstlane(seen) < 16u) {
      __builtin_amdgcn_s_sleep(2);
      seen = __hip_atomic_load(c, __ATOMIC_RELAXED, __HIP_MEMORY_SCOPE_AGENT);
      if (++spins > (1u << 22)) { timeout = true; break; }
    }
  };

  STR(15); ST(0);
  u32x4 X[36], wp0, wp1;
#pragma unroll
  for (int m = 0; m < 9; ++m) pieceX(0, m);
#pragma unroll
  for (int m = 0; m < 9; ++m) pieceQ(m / 3, m % 3);
  WAITV(3); BAR();
  const unsigned rx = opq(L0 + tg4 * 18432 + l16);
#pragma unroll
  for (int s = 0; s < 18; ++s) X[s] = lds_rd128(rx + s * 1024);
  WAITL0(); BAR();
  ST(1);

  f32x16 aq = {}, ak = {}, av = {};
  {
    const unsigned rq = L0 + 3 * SLOT + (4 * q4 + 2 * h) * 80 + ((72 * j) & 15) + 32 * p + 32 * g16 + 8 * pp;
#define QADDR(n) (rb[((n) / 18) & 1] + (((n) % 18) / 3) * 3840 + ((n) % 3) * 1280)
    u32x4 a[3];
    unsigned rb[2] = {rq, rq};
    sfor<0, 108>([&](auto nn) {
      constexpr int n = decltype(nn)::v, g = n / 18, r = n % 18;
      if (r == 0) {
        if (g == 1 || g == 4) WAITV(9);
        if (g == 3) WAITV(3);
        if (g) BAR();
        rb[(g + 1) & 1] = opq(rq + ((g + 1) % 3) * SLOT);
        if (g == 3) {
#pragma unroll
          for (int s = 18; s < 36; ++s) X[s] = lds_rd128(rx + (s - 18) * 1024);
          const unsigned wa = L0 + (p ? 3 : 5) * SLOT + 23040;
          wp0 = lds_rd128(wa + l16);
          const u32x2 w1 = lds_rd64(wa + 1024 + l * 8);
          wp1 = u32x4{w1[0], w1[1], 0u, 0u};
        }
        if (g == 0 || g == 3) {
#pragma unroll
          for (int e = 0; e < 3; ++e) a[e] = trfrag(QADDR(n + e), QADDR(n + e) + 80);
        }
        if (g == 3) { WAITL0(); BAR(); }
        SCHED();
      }
      if (g == 0 && r < 9) pieceX(1, r);
      if ((g == 1 || g == 2) && r < 3) pieceQ(g + 2, r);
      if (g == 3 && r < 3) pieceQ(5, r);
      if (g == 3 && r >= 3 && r < 12) pieceA2(0, (r - 3) / 3, r % 3);
      if (g >= 4 && r % 6 == 1) pieceA2(0, g - 1, r / 6);
      constexpr int s = n / 3;
      if (n % 3 == 0) aq = mfma32(a[0], X[s], aq);
      else if (n % 3 == 1) ak = mfma32(a[1], X[s], ak);
      else av = mfma32(X[s], a[2], av);
      if (n + 3 < 108 && (n + 3 < 54 || n >= 54)) a[n % 3] = trfrag(QADDR(n + 3), QADDR(n + 3) + 80);
      if (n == 107 || n == 53) asm volatile("" : "+v"(aq), "+v"(ak), "+v"(av));
      SCHED();
    });
  }
  ST(2);
  constexpr unsigned PQ_OFF = 5 * SLOT + 8 * 3840;
  {
    auto phi = [&](const f32x16& c) {
      u32x4 b0 = {cvtpk(c[0], c[1]), cvtpk(c[2], c[3]), cvtpk(c[4], c[5]), cvtpk(c[6], c[7])};
      u32x4 b1 = {cvtpk(c[8], c[9]), cvtpk(c[10], c[11]), 0u, 0u};
      b0[0] = (p && !h) ? 0u : b0[0];
      b1[0] = h ? 0u : b1[0];
      b1[1] = (p && !h) ? b1[1] : 0u;
      f32x16 z = {};
      z = mfma32(wp0, b0, z);
      return mfma32(wp1, b1, z);
    };
    const f32x16 pq = phi(aq), pk = phi(ak);
#pragma unroll
    for (int i = 0; i < 5; ++i) *(LAS unsigned*)(uintptr_t)(L0 + PQ_OFF + i * 2048 + tid * 4) = cvtpk(pq[2 * i], pq[2 * i + 1]);
    float pv[10];
#pragma unroll
    for (int i = 0; i < 5; ++i) { const unsigned u = cvtpk(pk[2 * i], pk[2 * i + 1]); pv[2 * i] = bf_lo(u); pv[2 * i + 1] = bf_hi(u); }
    u32x4 pos, neg; u32x2 ext;
    hedgehog(pv, h, pos, neg, ext);
    BAR();
    const unsigned R = L0 + 5 * SLOT + w * 3840, tk = l & 31;
    lds_wr64(R + tk * 64 + 8 * h, u32x2{pos[0], pos[1]});
    lds_wr64(R + tk * 64 + 16 + 8 * h, u32x2{pos[2], pos[3]});
    lds_wr64(R + tk * 64 + 32 + 8 * h, u32x2{neg[0], neg[1]});
    lds_wr64(R + tk * 64 + 48 + 8 * h, u32x2{neg[2], neg[3]});
    lds_wr64(R + 2048 + l * 8, ext);
    asm volatile("" ::: "memory");
    f32x16 kv0 = {}, kv1 = {};
#pragma unroll
    for (int s = 0; s < 2; ++s) {
      u32x4 vb;
#pragma unroll
      for (int i = 0; i < 4; ++i) vb[i] = (tk == 20) ? 0x3f803f80u : cvtpk(av[8 * s + 2 * i], av[8 * s + 2 * i + 1]);
      const unsigned r0 = 16 * s + 4 * h + q4;
      kv0 = mfma32(trfrag(R + r0 * 64 + 32 * g16 + 8 * pp, R + (r0 + 8) * 64 + 32 * g16 + 8 * pp), vb, kv0);
      kv1 = mfma32(trfrag(R + 2048 + r0 * 8, R + 2048 + (r0 + 8) * 8), vb, kv1);
    }
    asm volatile("" ::: "memory");
    if (tk < 24) {
      const unsigned Rl = R + (h * 24 + tk) * 16;
#pragma unroll
      for (int qd = 0; qd < 4; ++qd)
        lds_wr128(Rl + qd * 768, __builtin_bit_cast(u32x4, f32x4{kv0[4 * qd], kv0[4 * qd + 1], kv0[4 * qd + 2], kv0[4 * qd + 3]}));
      lds_wr128(Rl + 3072, __builtin_bit_cast(u32x4, f32x4{kv1[0], kv1[1], kv1[2], kv1[3]}));
    }
    WAITL0(); WAITV(9); BAR();
    if (tid < 480) {
      const int pq_ = tid >= 240, rem = tid - 240 * pq_;
      f32x4 sum = {};
#pragma unroll
      for (int e = 0; e < 4; ++e) sum += __builtin_bit_cast(f32x4, lds_rd128(L0 + 5 * SLOT + (2 * e + pq_) * 3840 + rem * 16));
      __builtin_amdgcn_raw_buffer_store_b128(__builtin_bit_cast(u32x4, sum),
          __builtin_amdgcn_make_buffer_rsrc(ws + WS_KVP, 0, 32 * 16 * 3840, 0x00020000),
          ((2 * j + pq_) * 16 + t) * 3840 + rem * 16, 0, 16);
    }
    WAITL0();
  }

  ST(3);
  u32x4 H[18];
  {
    u32x4 a[3], hh[9];
    auto act = [&](const f32x16& c) {
      u32x4 r;
#pragma unroll
      for (int i = 0; i < 4; ++i) r[i] = cvtpk(silu_f(c[2 * i]) * c[8 + 2 * i], silu_f(c[2 * i + 1]) * c[9 + 2 * i]);
      return r;
    };
    const unsigned rA2 = L0 + h * 2048 + g16 * 1024 + q4 * 128 + (((2 * p) ^ ((q4 >> 1) + 2 * g16)) * 32) + pp * 8;
    const auto kvp = __builtin_amdgcn_make_buffer_rsrc(ws + WS_KVP, 0, 32 * 16 * 3840, 0x00020000);
    const auto kvf = __builtin_amdgcn_make_buffer_rsrc(ws + WS_KVF + j * 4096, 0, 4096, 0x00020000);
#pragma unroll
    for (int e = 0; e < 9; ++e) hh[e] = u32x4{0u, 0u, 0u, 0u};
#pragma unroll
    for (int e = 0; e < 3; ++e) { const unsigned ad = (rA2 ^ ((e & 1) * 32)) + (e >> 1) * 4096; a[e] = trfrag(ad, ad + 512); }
#pragma unroll 1
    for (int v = 0; v < 4; ++v) {
      f32x16 c0 = {}, c1 = {};
      unsigned rb[2][2], seen = 0;
      u32x4 kl = {};
      rb[0][0] = opq(rA2); rb[0][1] = opq(rA2 ^ 32);
      sfor<0, 72>([&](auto nn) {
        constexpr int n = decltype(nn)::v, i = n / 12, r = n % 12;
        if (r == 0) {
          if ((i == 1 && v == 0) || (i == 0 && v == 2)) WAITV(0); else WAITV(9);
          BAR();
          if (i == 1 && v == 0 && tid == 0) __hip_atomic_fetch_add(cnt1, 1u, __ATOMIC_RELAXED, __HIP_MEMORY_SCOPE_AGENT);
          if (i == 0 && v == 2 && tid == 0) __hip_atomic_fetch_add(cnt2, 1u, __ATOMIC_RELAXED, __HIP_MEMORY_SCOPE_AGENT);
          if (v == 1) {
            if (i == 0) seen = __hip_atomic_load(cnt1, __ATOMIC_RELAXED, __HIP_MEMORY_SCOPE_AGENT);
            if (i == 2) {
              spin16(cnt1, seen);
              const int I = 30 * t + min(tid >> 4, 29), pq_ = I >= 240;
              kl = __builtin_amdgcn_raw_buffer_load_b128(kvp, ((2 * j + pq_) * 16 + (tid & 15)) * 3840 + (I - 240 * pq_) * 16, 0, 16);
            }
            if (i == 4) {
              f32x4 sm = __builtin_bit_cast(f32x4, kl);
#pragma unroll
              for (int m = 1; m < 16; m <<= 1)
#pragma unroll
                for (int e = 0; e < 4; ++e) sm[e] += __shfl_xor(sm[e], m);
              if (timeout) sm[0] = __uint_as_float(0x7fc00000u);
              if ((tid & 15) == 0 && tid < 480)
                __builtin_amdgcn_raw_buffer_store_b64(u32x2{cvtpk(sm[0], sm[1]), cvtpk(sm[2], sm[3])}, kvf, (30 * t + (tid >> 4)) * 8, 0, 16);
            }
          }
          if (i < 5) { rb[(i + 1) & 1][0] = opq(rA2 + (i + 1) * SLOT); rb[(i + 1) & 1][1] = opq((rA2 ^ 32) + (i + 1) * SLOT); }
          else { rb[0][0] = opq(rA2); rb[0][1] = opq(rA2 ^ 32); }
          SCHED();
        }
        if (r % 4 == 1) {
          constexpr int m = r / 4;
          if (i == 0) pieceA2(v, 5, m);
          else if (v < 3) pieceA2(v + 1, i - 1, m);
          else if (i < 4) pieceA1(i - 1, m);
          else pieceB(i - 4, L0 + (i - 1) * SLOT, m);
        }
        if (n % 2 == 0) c0 = mfma32(a[n % 3], X[n / 2], c0); else c1 = mfma32(a[n % 3], X[n / 2], c1);
        constexpr int q = n + 3;
        const unsigned ad = rb[(q / 12) & 1][q & 1] + ((q % 12) >> 1) * 4096;
        a[n % 3] = trfrag(ad, ad + 512);
        SCHED();
      });
#pragma unroll
      for (int e = 0; e < 6; ++e) hh[e] = hh[e + 2];
      hh[6] = act(c0); hh[7] = act(c1);
    }
    l = opq(__builtin_amdgcn_mbcnt_hi(~0u, __builtin_amdgcn_mbcnt_lo(~0u, 0u)));
    h = l >> 5; g16 = (l >> 4) & 1; q4 = (l >> 2) & 3; pp = l & 3; l16 = l * 16;
    const unsigned rA1 = L0 + g16 * 1024 + h * 512 + q4 * 64 + (p ^ g16) * 32 + pp * 8;
    u32x2 kc[5];
    {
      const unsigned r0 = opq(rA1), r2 = opq(rA1 + 2 * SLOT);
      unsigned seen = __hip_atomic_load(cnt2, __ATOMIC_RELAXED, __HIP_MEMORY_SCOPE_AGENT);
      f32x16 c0 = {};
      sfor<0, 36>([&](auto nn) {
        constexpr int s = decltype(nn)::v;
        if (s == 0) {
#pragma unroll
          for (int e = 0; e < 3; ++e) a[e] = trfrag(r0 + e * 2048, r0 + e * 2048 + 256);
          WAITV(9); BAR();
        }
        if (s == 12) {
          WAITV(9); BAR();
          spin16(cnt2, seen);
          const unsigned ka = (p * 240 + h * 24 + min(l & 31, 23)) * 8;
#pragma unroll
          for (int e = 0; e < 5; ++e) kc[e] = __builtin_amdgcn_raw_buffer_load_b64(kvf, ka + e * 384, 0, 16);
          SCHED();
        }
        if (s < 12 && s % 4 == 1) pieceB(2, L0 + 5 * SLOT, s / 4);
        c0 = mfma32(a[s % 3], X[s], c0);
        constexpr int q = s + 3;
        if (q < 36) { const unsigned ad = q < 24 ? r0 + q * 2048 : r2 + (q - 24) * 2048; a[s % 3] = trfrag(ad, ad + 256); }
        SCHED();
      });
      hh[8] = act(c0);
    }
    ST(4);
    BAR();
    ST(5);
#pragma unroll
    for (int e = 0; e < 8; ++e) lds_wr128(L0 + (tg4 * 18 + 2 * jo + 4 * (e >> 1) + 2 * p + (e & 1)) * 1024 + l16, hh[e]);
    lds_wr128(L0 + (tg4 * 18 + 16 * (1 - jo) + p) * 1024 + l16, hh[8]);
    WAITL0(); BAR();
#pragma unroll
    for (int s = 0; s < 18; ++s) H[s] = lds_rd128(L0 + tg4 * 18432 + l16 + s * 1024);
    const u32x4 k0 = {kc[0][0], kc[0][1], kc[1][0], kc[1][1]}, k1 = {kc[2][0], kc[2][1], kc[3][0], kc[3][1]};
    const u32x4 k2 = {h ? 0u : kc[4][0], h ? 0u : kc[4][1], 0u, 0u};
    WAITL0();
    WAITV(0); BAR();
#pragma unroll
    for (int m = 0; m < 9; ++m) pieceB(3 + m / 3, L0 + (m / 3) * SLOT, m % 3);
    ST(6);
    float pv[10];
#pragma unroll
    for (int i = 0; i < 5; ++i) {
      const unsigned u = *(LAS unsigned*)(uintptr_t)(L0 + PQ_OFF + i * 2048 + w * 256 + l * 4);
      pv[2 * i] = bf_lo(u); pv[2 * i + 1] = bf_hi(u);
    }
    u32x4 pos, neg; u32x2 ext;
    hedgehog(pv, h, pos, neg, ext);
    f32x16 on = {};
    on = mfma32(k0, pos, on); on = mfma32(k1, neg, on); on = mfma32(k2, u32x4{ext[0], ext[1], 0u, 0u}, on);
    const float inv = timeout ? __uint_as_float(0x7fc00000u) : __builtin_amdgcn_rcpf(xhalf_sum(h ? on[8] : 0.f) + 1e-6f);
    char* op = ws + WS_O + (size_t)(32 * tg + (l & 31)) * 1152 + (36 * j + 16 * p + 4 * h) * 2;
    const u32x2 o0 = {cvtpk(on[0] * inv, on[1] * inv), cvtpk(on[2] * inv, on[3] * inv)};
    const u32x2 o1 = {cvtpk(on[4] * inv, on[5] * inv), cvtpk(on[6] * inv, on[7] * inv)};
    const u32x2 o2 = {cvtpk(on[8] * inv, on[9] * inv), cvtpk(on[10] * inv, on[11] * inv)};
    if (p && !h) *(unsigned*)(op + 4) = o0[1]; else *(u32x2*)op = o0;
    *(u32x2*)(op + 16) = o1;
    if (!h) { if (p) *(u32x2*)(op + 32) = o2; else *(unsigned*)(op + 32) = o2[0]; }
  }

  ST(7);
  {
    unsigned cur = L0 + HALF, oth = L0;
    const unsigned rB = h * 512 + p * 256 + q4 * 64 + g16 * 32 + pp * 8;
    const auto yr = __builtin_amdgcn_make_buffer_rsrc(ws + WS_YP, 0, (unsigned)YP_BYTES, 0x00020000);
    const unsigned ybase = ((j * 64 + tg) * 36 + 2 * p) * 1024;
    u32x4 a[3];
#pragma unroll
    for (int e = 0; e < 3; ++e) a[e] = trfrag(cur + rB + e * 2048, cur + rB + e * 2048 + 1024);
#pragma unroll 1
    for (int P = 0; P < 5; ++P) {
      const unsigned rc = opq(cur + rB), rc2 = opq(cur + rB + 2 * SLOT), rn = opq(oth + rB);
      f32x16 acc = {};
#pragma unroll
      for (int n = 0; n < 36; ++n) {
        if (n == 18 && P == 4) break;
        if (n % 12 == 0 && !(n == 0 && P == 0)) {
          const int i = n / 12, gb = 3 * P + i;
          if (gb < 9) WAITV(9); else if (gb < 13) WAITV(2);
          BAR();
        }
        if (n % 4 == DPOS) {
          const int i = n / 12, gb = 3 * P + i;
          if (gb + 5 <= 13 && gb > 0) pieceB(gb + 5, i == 0 ? oth + 2 * SLOT : cur + (i - 1) * SLOT, (n % 12) / 4);
        }
        acc = mfma32(a[n % 3], H[n % 18], acc);
        const int sn = n + 3;
        const unsigned ad = sn < 24 ? rc + sn * 2048 : sn < 36 ? rc2 + (sn - 24) * 2048 : rn + (sn - 36) * 2048;
        a[n % 3] = trfrag(ad, ad + 1024);
        SCHED();
        if (n % 18 == 17) {
          const unsigned so = ybase + (2 * P + n / 18) * 4096;
#pragma unroll
          for (int e = 0; e < 2; ++e) {
            const u32x4 y = {cvtpk(acc[8 * e], acc[8 * e + 1]), cvtpk(acc[8 * e + 2], acc[8 * e + 3]),
                             cvtpk(acc[8 * e + 4], acc[8 * e + 5]), cvtpk(acc[8 * e + 6], acc[8 * e + 7])};
            __builtin_amdgcn_raw_buffer_store_b128(y, yr, l16 + e * 1024, so, YAUX);
          }
          acc = f32x16{};
        }
      }
      const unsigned tmp = cur; cur = oth; oth = tmp;
    }
  }
  ST(8);
  ST(9); ST(13); STR(14);
}

__global__ __launch_bounds__(576, 3) void k_final(const unsigned short* __restrict__ x, const char* __restrict__ ws,
                                                  unsigned short* __restrict__ out) {
  __shared__ __attribute__((aligned(1024))) char lds[36864];
  const unsigned L0 = (unsigned)(uintptr_t)(LAS char*)lds;
  const int b = blockIdx.x, tg = (b & 7) * 8 + (b >> 5), dq = (b >> 3) & 3;
  const int w = __builtin_amdgcn_readfirstlane(threadIdx.x >> 6), l = threadIdx.x & 63, g = l >> 4, hb = 9 * dq + w;
  const i32x4 dO = mkdesc(ws + WS_O, S_ * D_ * 2);
#pragma unroll
  for (int m = 0; m < 4; ++m) {
    const int fi = 4 * w + m, tb = fi >= 18, s = fi - 18 * tb;
    dma16(L0 + fi * 1024, (l & 15) * 1152 + g * 16, dO, (32 * tg + 16 * tb) * 1152 + 64 * s);
  }
  u32x4 wf[18];
  const u32x4* wsrc = (const u32x4*)(ws + WS_WOT + (size_t)hb * 18432) + l;
#pragma unroll
  for (int s = 0; s < 18; ++s) wf[s] = wsrc[s * 64];
  const size_t xo = (size_t)(32 * tg + (l & 15)) * D_ + 16 * hb + 8 * (g & 1) + 4 * (g >> 1);
  const u32x2 x0 = *(const u32x2*)(x + xo), x1 = *(const u32x2*)(x + xo + 16 * D_);
  u32x2 yp[16][2];
  const char* ysrc = ws + WS_YP + (size_t)(tg * 36 + hb) * 1024 + ((g >> 1) * 32 + (l & 15)) * 16 + (g & 1) * 8;
#pragma unroll
  for (int jj = 0; jj < 16; ++jj) {
    yp[jj][0] = *(const u32x2*)(ysrc + (size_t)jj * (64 * 36 * 1024));
    yp[jj][1] = *(const u32x2*)(ysrc + (size_t)jj * (64 * 36 * 1024) + 256);
  }
  WAITV(52); BAR();
  f32x4 s0 = {}, s1 = {};
#pragma unroll
  for (int s = 0; s < 18; ++s) {
    s0 = mfma16(wf[s], lds_rd128(L0 + s * 1024 + l * 16), s0);
    s1 = mfma16(wf[s], lds_rd128(L0 + (18 + s) * 1024 + l * 16), s1);
  }
  f32x4 y0 = {}, y1 = {};
#pragma unroll
  for (int jj = 0; jj < 16; ++jj) {
    y0 += f32x4{bf_lo(yp[jj][0][0]), bf_hi(yp[jj][0][0]), bf_lo(yp[jj][0][1]), bf_hi(yp[jj][0][1])};
    y1 += f32x4{bf_lo(yp[jj][1][0]), bf_hi(yp[jj][1][0]), bf_lo(yp[jj][1][1]), bf_hi(yp[jj][1][1])};
  }
  const u32x2 r0 = {cvtpk(bf_lo(x0[0]) + s0[0] * silu_f(y0[0]), bf_hi(x0[0]) + s0[1] * silu_f(y0[1])),
                    cvtpk(bf_lo(x0[1]) + s0[2] * silu_f(y0[2]), bf_hi(x0[1]) + s0[3] * silu_f(y0[3]))};
  const u32x2 r1 = {cvtpk(bf_lo(x1[0]) + s1[0] * silu_f(y1[0]), bf_hi(x1[0]) + s1[1] * silu_f(y1[1])),
                    cvtpk(bf_lo(x1[1]) + s1[2] * silu_f(y1[2]), bf_hi(x1[1]) + s1[3] * silu_f(y1[3]))};
  *(u32x2*)(out + xo) = r0;
  *(u32x2*)(out + xo + 16 * D_) = r1;
}

extern "C" void kernel_launch(void* const* d_in, const int* in_sizes, int n_in, void* d_out, int out_size, void* d_ws,
                              size_t ws_size, hipStream_t stream) {
  const unsigned short* x = (const unsigned short*)d_in[0];
  const unsigned short* wqkv = (const unsigned short*)d_in[1];
  const unsigned short* wphi = (const unsigned short*)d_in[2];
  const unsigned short* wo = (const unsigned short*)d_in[3];
  const unsigned short* wg = (const unsigned short*)d_in[5];
  const unsigned short* wu = (const unsigned short*)d_in[6];
  const unsigned short* wd = (const unsigned short*)d_in[7];
  char* ws = (char*)d_ws;
  k_prep<<<256, 512, 0, stream>>>(x, wqkv, wo, wphi, ws);
  k_main<<<256, 512, 0, stream>>>(wg, wu, wd, ws);
  k_final<<<256, 576, 0, stream>>>(x, ws, (unsigned short*)d_out);
}
```

```cpp
#define YAUX 16
#define YLD 2
#include <hip/hip_runtime.h>
#include <hip/hip_bf16.h>
#include <stdint.h>

typedef __attribute__((ext_vector_type(8))) short s16x8;
typedef __attribute__((ext_vector_type(4))) short s16x4;
typedef __attribute__((ext_vector_type(8))) __bf16 bf16x8;
typedef __attribute__((ext_vector_type(2))) __bf16 bf16x2;
typedef __attribute__((ext_vector_type(16))) float f32x16;
typedef __attribute__((ext_vector_type(4))) float f32x4;
typedef __attribute__((ext_vector_type(2))) float f32x2;
typedef __attribute__((ext_vector_type(4))) unsigned u32x4;
typedef __attribute__((ext_vector_type(2))) unsigned u32x2;
typedef __attribute__((ext_vector_type(4))) int i32x4;
#define LAS __attribute__((address_space(3)))
#define GAS __attribute__((address_space(1)))
#define DEV __device__ __forceinline__

template <int I> struct IC { static constexpr int v = I; };
template <int B, int E, class Fn> DEV void sfor(Fn&& fn) {
  if constexpr (B < E) { fn(IC<B>{}); sfor<B + 1, E>(fn); }
}

constexpr int S_ = 2048, D_ = 576, F_ = 4608;
constexpr size_t WS_XN = 0;
constexpr size_t WS_WOT = WS_XN + 64 * 36 * 1024;
constexpr size_t WS_KVP = WS_WOT + 36 * 18 * 1024;
constexpr size_t WS_O = WS_KVP + 32 * 16 * 3840;
constexpr size_t WS_CNT = WS_O + 2048 * 576 * 2;
constexpr size_t WS_KVF = WS_CNT + 32 * 256;
constexpr size_t WS_QI = WS_KVF + 16 * 4096;
constexpr size_t WS_YP = WS_QI + 16 * 6 * 24576;
constexpr size_t YP_BYTES = (size_t)16 * 64 * 36 * 1024;

DEV unsigned cvtpk(float lo, float hi) {
  f32x2 v = {lo, hi};
  return __builtin_bit_cast(unsigned, __builtin_convertvector(v, bf16x2));
}
DEV float bf_lo(unsigned u) { return __uint_as_float(u << 16); }
DEV float bf_hi(unsigned u) { return __uint_as_float(u & 0xffff0000u); }
DEV f32x16 mfma32(u32x4 a, u32x4 b, f32x16 c) {
  return __builtin_amdgcn_mfma_f32_32x32x16_bf16(__builtin_bit_cast(bf16x8, a), __builtin_bit_cast(bf16x8, b), c, 0, 0, 0);
}
DEV f32x4 mfma16(u32x4 a, u32x4 b, f32x4 c) {
  return __builtin_amdgcn_mfma_f32_16x16x32_bf16(__builtin_bit_cast(bf16x8, a), __builtin_bit_cast(bf16x8, b), c, 0, 0, 0);
}
DEV u32x2 trd(unsigned addr) {
  return __builtin_bit_cast(u32x2, __builtin_amdgcn_ds_read_tr16_b64_v4i16((LAS s16x4*)(uintptr_t)addr));
}
DEV u32x4 trfrag(unsigned a0, unsigned a1) {
  u32x2 lo = trd(a0), hi = trd(a1);
  u32x4 r = {lo[0], lo[1], hi[0], hi[1]};
  return r;
}
DEV u32x4 lds_rd128(unsigned a) { return *(LAS u32x4*)(uintptr_t)a; }
DEV u32x2 lds_rd64(unsigned a) { return *(LAS u32x2*)(uintptr_t)a; }
DEV void lds_wr128(unsigned a, u32x4 v) { *(LAS u32x4*)(uintptr_t)a = v; }
DEV void lds_wr64(unsigned a, u32x2 v) { *(LAS u32x2*)(uintptr_t)a = v; }
DEV i32x4 mkdesc(const void* p, unsigned bytes) {
  uint64_t a = (uint64_t)p;
  i32x4 d = {(int)__builtin_amdgcn_readfirstlane((unsigned)a), (int)__builtin_amdgcn_readfirstlane((unsigned)(a >> 32) & 0xffffu),
             (int)bytes, 0x00020000};
  return d;
}
DEV void dma16(unsigned lds, unsigned voff, i32x4 desc, unsigned soff) {
  asm volatile("s_mov_b32 m0, %0\n\ts_nop 0\n\tbuffer_load_dwordx4 %1, %2, %3 offen lds"
               :: "s"(lds), "v"(voff), "s"(desc), "s"(soff) : "memory");
}
template <unsigned OFF> DEV void dma16i(unsigned lds, unsigned voff, i32x4 desc, unsigned soff) {
  asm volatile("s_add_u32 m0, %0, %4\n\ts_nop 0\n\tbuffer_load_dwordx4 %1, %2, %3 offen lds"
               :: "s"(lds), "v"(voff), "s"(desc), "s"(soff), "i"(OFF) : "memory", "scc");
}
#define WAITV(n) asm volatile("s_waitcnt vmcnt(" #n ")" ::: "memory")
#define ST(k)
#define STR(k)
#define SCHED() __builtin_amdgcn_sched_barrier(0)
DEV unsigned opq(unsigned v) { asm volatile("" : "+v"(v)); return v; }
#define WAITL0() asm volatile("s_waitcnt lgkmcnt(0)" ::: "memory")
#define BAR() do { asm volatile("" ::: "memory"); __builtin_amdgcn_s_barrier(); asm volatile("" ::: "memory"); } while (0)
DEV float xhalf_max(float x) {
  auto t = __builtin_amdgcn_permlane32_swap(__float_as_uint(x), __float_as_uint(x), false, false);
  return fmaxf(__uint_as_float(t[0]), __uint_as_float(t[1]));
}
DEV float xhalf_sum(float x) {
  auto t = __builtin_amdgcn_permlane32_swap(__float_as_uint(x), __float_as_uint(x), false, false);
  return __uint_as_float(t[0]) + __uint_as_float(t[1]);
}
constexpr float LOG2E = 1.4426950408889634f;
DEV float silu_f(float g) { return g * __builtin_amdgcn_rcpf(1.0f + __builtin_amdgcn_exp2f(-LOG2E * g)); }

DEV void hedgehog(const float (&pv)[10], int h, u32x4& pos, u32x4& neg, u32x2& ext) {
  float mx = pv[0], mn = pv[0];
#pragma unroll
  for (int r = 1; r < 8; ++r) { mx = fmaxf(mx, pv[r]); mn = fminf(mn, pv[r]); }
  if (h == 0) { mx = fmaxf(mx, fmaxf(pv[8], pv[9])); mn = fminf(mn, fminf(pv[8], pv[9])); }
  mx = xhalf_max(mx); mn = -xhalf_max(-mn);
  float ep[10], en[10], sp = 0.f, sn = 0.f;
#pragma unroll
  for (int r = 0; r < 10; ++r) {
    ep[r] = __builtin_amdgcn_exp2f((pv[r] - mx) * LOG2E);
    en[r] = __builtin_amdgcn_exp2f((mn - pv[r]) * LOG2E);
    if (r >= 8) { ep[r] = h ? 0.f : ep[r]; en[r] = h ? 0.f : en[r]; }
    sp += ep[r]; sn += en[r];
  }
  float ip = __builtin_amdgcn_rcpf(xhalf_sum(sp)), in = __builtin_amdgcn_rcpf(xhalf_sum(sn));
#pragma unroll
  for (int i = 0; i < 4; ++i) {
    pos[i] = cvtpk(ep[2 * i] * ip, ep[2 * i + 1] * ip);
    neg[i] = cvtpk(en[2 * i] * in, en[2 * i + 1] * in);
  }
  ext[0] = cvtpk(ep[8] * ip, ep[9] * ip);
  ext[1] = cvtpk(en[8] * in, en[9] * in);
}

__global__ __launch_bounds__(512) void k_prep(const unsigned short* __restrict__ x, const unsigned short* __restrict__ wqkv,
                                              const unsigned short* __restrict__ wo,
                                              const unsigned short* __restrict__ wphi, char* __restrict__ ws) {
  const int b = blockIdx.x, w = threadIdx.x >> 6, l = threadIdx.x & 63;
  if (w < 2) {
    const int tok = 8 * b + 4 * w + (l >> 4), i = l & 15;
    u32x2 v[9];
#pragma unroll
    for (int u = 0; u < 9; ++u) v[u] = *(const u32x2*)(x + (size_t)tok * D_ + 4 * (i + 16 * u));
    float f[36], sum = 0.f;
#pragma unroll
    for (int u = 0; u < 9; ++u) {
      f[4 * u] = bf_lo(v[u][0]); f[4 * u + 1] = bf_hi(v[u][0]); f[4 * u + 2] = bf_lo(v[u][1]); f[4 * u + 3] = bf_hi(v[u][1]);
      sum += (f[4 * u] + f[4 * u + 1]) + (f[4 * u + 2] + f[4 * u + 3]);
    }
#pragma unroll
    for (int m = 1; m < 16; m <<= 1) sum += __shfl_xor(sum, m);
    const float mean = sum * (1.0f / D_);
    float sq = 0.f;
#pragma unroll
    for (int e = 0; e < 36; ++e) { f[e] -= mean; sq += f[e] * f[e]; }
#pragma unroll
    for (int m = 1; m < 16; m <<= 1) sq += __shfl_xor(sq, m);
    const float rs = 1.0f / sqrtf(sq * (1.0f / D_) + 1e-5f);
    char* dst = ws + WS_XN + (size_t)(tok >> 5) * 36864 + (tok & 31) * 16;
#pragma unroll
    for (int u = 0; u < 9; ++u) {
      const int c8 = i + 16 * u, c = c8 >> 1;
      u32x2 o = {cvtpk(f[4 * u] * rs, f[4 * u + 1] * rs), cvtpk(f[4 * u + 2] * rs, f[4 * u + 3] * rs)};
      *(u32x2*)(dst + (c >> 1) * 1024 + (c & 1) * 512 + (c8 & 1) * 8) = o;
    }
  } else if (w >= 5) {
    u32x4 v[3];
#pragma unroll
    for (int m = 0; m < 3; ++m) {
      const int id = 576 * b + (int)threadIdx.x - 320 + 192 * m, jj = id / 9216, g = (id % 9216) / 1536;
      const int c = min(id % 1536, 1439), k16 = c / 240, r = c % 240, seg = r / 80, pos = (r % 80) / 5, ch = r % 5;
      const int row = 96 * g + 16 * k16 + 8 * ((pos >> 1) & 1) + 4 * (pos & 1) + (pos >> 2);
      const int off = min(row * 3456 + seg * 1152 + ((72 * jj) & ~15) + 16 * ch, D_ * 3 * D_ * 2 - 16);
      v[m] = *(const u32x4*)((const char*)wqkv + off);
    }
#pragma unroll
    for (int m = 0; m < 3; ++m) {
      const int id = 576 * b + (int)threadIdx.x - 320 + 192 * m, g = (id % 9216) / 1536;
      if (!(id % 1536 >= 1440 && (g == 2 || g == 3))) *(u32x4*)(ws + WS_QI + (size_t)id * 16) = v[m];
    }
  } else {
    for (int fi = 3 * b + (w - 2); fi < 648 + 64; fi += 768) {
      unsigned short e[8];
      char* dst;
      if (fi < 648) {
        const int hb = fi / 18, s = fi % 18, i = l & 15, g = l >> 4, gi = i >> 2;
        const int d = 16 * hb + 8 * (gi & 1) + 4 * (gi >> 1) + (i & 3);
#pragma unroll
        for (int jj = 0; jj < 8; ++jj) e[jj] = wo[(size_t)(32 * s + 8 * g + jj) * D_ + d];
        dst = ws + WS_WOT + (size_t)fi * 1024;
      } else {
        const int q = fi - 648, head = q >> 1, s = q & 1, f = l & 31, h = l >> 5;
#pragma unroll
        for (int jj = 0; jj < 8; ++jj) {
          const int d = 16 * s + 8 * (jj >> 2) + 4 * h + (jj & 3) - 2 * (head & 1);
          const bool ok = f < 18 && d >= 0 && d < 18;
          const unsigned short t = wphi[head * 324 + (ok ? d * 18 + f : 0)];
          e[jj] = ok ? t : (unsigned short)0;
        }
        dst = ws + WS_QI + (size_t)((head >> 1) * 6 + 2 + (head & 1)) * 24576 + 23040;
        if (s) {
          *(u32x2*)(dst + 1024 + l * 8) = u32x2{e[0] | ((unsigned)e[1] << 16), e[2] | ((unsigned)e[3] << 16)};
          continue;
        }
      }
      u32x4 o = {e[0] | ((unsigned)e[1] << 16), e[2] | ((unsigned)e[3] << 16), e[4] | ((unsigned)e[5] << 16), e[6] | ((unsigned)e[7] << 16)};
      *(u32x4*)(dst + l * 16) = o;
    }
  }
  if (b == 0 && threadIdx.x < 32) *(unsigned*)(ws + WS_CNT + threadIdx.x * 256) = 0u;
}

constexpr unsigned SLOT = 24576, HALF = 3 * SLOT, SPARE = 6 * SLOT, LDS_MAIN = 163840;
#define JMAP (2 * (b & 7) + ((b >> 3) & 1))
#define TMAP (b >> 4)
#define NA 4
#define NB 6
#define NQ 6

__global__ __launch_bounds__(512, 2) void k_main(const unsigned short* __restrict__ wg, const unsigned short* __restrict__ wu,
                                                 const unsigned short* __restrict__ wd, char* __restrict__ ws) {
  __shared__ __attribute__((aligned(1024))) char lds[LDS_MAIN];
  const unsigned L0 = (unsigned)(uintptr_t)(LAS char*)lds;
  const int b = blockIdx.x, tid = threadIdx.x;
  const int j = JMAP, t = TMAP, jo = j & 1;
  const int w = __builtin_amdgcn_readfirstlane(tid >> 6), tg4 = w >> 1, p = w & 1;
  int l = tid & 63, h = l >> 5, g16 = (l >> 4) & 1, q4 = (l >> 2) & 3, pp = l & 3;
  const int tg = 4 * t + tg4;
  GAS unsigned* const cnt1 = (GAS unsigned*)(ws + WS_CNT + j * 256);
  GAS unsigned* const cnt2 = cnt1 + 1024;

  const i32x4 dW = mkdesc(ws, (unsigned)WS_YP), dA = mkdesc(p ? wu : wg, D_ * F_ * 2), dB = mkdesc(wd, D_ * F_ * 2);
  unsigned l16 = l * 16;
  const unsigned vA2 = (l >> 3) * 9216 + ((((l >> 1) & 3) ^ (((l >> 4) & 1) + 2 * p)) * 32) + (l & 1) * 16;
  unsigned vA1 = 0, vB = 0;
  auto pieceX = [&](int half, int m) {
    const int n = 9 * w + m, tq = w >> 1, s = 18 * half + n - 18 * tq;
    dma16(L0 + n * 1024, l16, dW, (unsigned)WS_XN + ((4 * t + tq) * 36 + s) * 1024);
  };
  auto pieceQ = [&](int g, int m) {
    dma16(L0 + (3 + g % 3) * SLOT + (w + 8 * m) * 1024, l16, dW, (unsigned)WS_QI + (j * 6 + g) * SLOT + (w + 8 * m) * 1024);
  };
  auto pieceA2 = [&](int v, int i, int m) {
    dma16(L0 + i * SLOT + (w + 8 * m) * 1024, vA2, dA,
          (96 * i + 16 * ((w >> 2) + 2 * m) + 8 * (tg4 & 1)) * 9216 + (288 * j + 32 * jo + 64 * v) * 2);
  };
  auto pieceA1 = [&](int i, int m) {
    dma16(L0 + i * SLOT + (w + 8 * m) * 1024, vA1, dA, (192 * i + 16 * (tg4 + 4 * m)) * 9216 + (288 * j + 256 * (1 - jo)) * 2);
  };
  const bool grp = w >= 4;
  const unsigned wl = L0 + w * 1024;
  const unsigned sA2 = (16 * (w >> 2) + 8 * (tg4 & 1)) * 9216 + (288 * j + 32 * jo) * 2;
  unsigned sB[9];
#pragma unroll
  for (int e = 0; e < 9; ++e) {
    const int it = 12 * (e / 3) + tg4 + 4 * (e % 3), dp = it >= 18;
    sB[e] = (288 * j + 16 * (it - 18 * dp)) * 1152 + 128 * dp;
  }
  bool timeout = false;
  auto spin16 = [&](GAS unsigned* c, unsigned seen) {
    unsigned spins = 0;
    while (__builtin_amdgcn_readfirstlane(seen) < 16u) {
      __builtin_amdgcn_s_sleep(2);
      seen = __hip_atomic_load(c, __ATOMIC_RELAXED, __HIP_MEMORY_SCOPE_AGENT);
      if (++spins > (1u << 22)) { timeout = true; break; }
    }
  };

  STR(15); ST(0);
  u32x4 X[36], wp0, wp1;
#pragma unroll
  for (int m = 0; m < 9; ++m) pieceX(0, m);
#pragma unroll
  for (int m = 0; m < 9; ++m) pieceQ(m / 3, m % 3);
  WAITV(3); BAR();
  const unsigned rx = opq(L0 + tg4 * 18432 + l16);
#pragma unroll
  for (int s = 0; s < 18; ++s) X[s] = lds_rd128(rx + s * 1024);
  WAITL0(); BAR();
  ST(1);

  f32x16 aq = {}, ak = {}, av = {};
  {
    const unsigned rq = L0 + 3 * SLOT + (4 * q4 + 2 * h) * 80 + ((72 * j) & 15) + 32 * p + 32 * g16 + 8 * pp;
#define QADDR(n) (rb[((n) / 18) & 1] + (((n) % 18) / 3) * 3840 + ((n) % 3) * 1280)
    u32x4 a[NQ];
    unsigned rb[2] = {rq, rq};
    sfor<0, 108>([&](auto nn) {
      constexpr int n = decltype(nn)::v, g = n / 18, r = n % 18;
      if (r == 0) {
        if (g == 1 || g == 4) WAITV(9);
        if (g == 3) WAITV(3);
        if (g) BAR();
        rb[(g + 1) & 1] = opq(rq + ((g + 1) % 3) * SLOT);
        if (g == 3) {
#pragma unroll
          for (int s = 18; s < 36; ++s) X[s] = lds_rd128(rx + (s - 18) * 1024);
          const unsigned wa = L0 + (p ? 3 : 5) * SLOT + 23040;
          wp0 = lds_rd128(wa + l16);
          const u32x2 w1 = lds_rd64(wa + 1024 + l * 8);
          wp1 = u32x4{w1[0], w1[1], 0u, 0u};
        }
        if (g == 0 || g == 3) {
#pragma unroll
          for (int e = 0; e < NQ; ++e) a[e] = trfrag(QADDR(n + e), QADDR(n + e) + 80);
        }
        if (g == 3) { WAITL0(); BAR(); }
        SCHED();
      }
      if (g == 0 && r < 9) pieceX(1, r);
      if ((g == 1 || g == 2) && r < 3) pieceQ(g + 2, r);
      if (g == 3 && r < 3) pieceQ(5, r);
      if (g == 3 && r >= 3 && r < 12) pieceA2(0, (r - 3) / 3, r % 3);
      if (g >= 4 && r % 6 == 1) pieceA2(0, g - 1, r / 6);
      constexpr int s = n / 3;
      if (n % 3 == 0) aq = mfma32(a[n % NQ], X[s], aq);
      else if (n % 3 == 1) ak = mfma32(a[n % NQ], X[s], ak);
      else av = mfma32(X[s], a[n % NQ], av);
      if (n + NQ < 108 && (n + NQ < 54 || n >= 54)) a[n % NQ] = trfrag(QADDR(n + NQ), QADDR(n + NQ) + 80);
      if (n == 107 || n == 53) asm volatile("" : "+v"(aq), "+v"(ak), "+v"(av));
      SCHED();
    });
  }
  ST(2);
  constexpr unsigned PQ_OFF = 5 * SLOT + 8 * 3840;
  {
    auto phi = [&](const f32x16& c) {
      u32x4 b0 = {cvtpk(c[0], c[1]), cvtpk(c[2], c[3]), cvtpk(c[4], c[5]), cvtpk(c[6], c[7])};
      u32x4 b1 = {cvtpk(c[8], c[9]), cvtpk(c[10], c[11]), 0u, 0u};
      b0[0] = (p && !h) ? 0u : b0[0];
      b1[0] = h ? 0u : b1[0];
      b1[1] = (p && !h) ? b1[1] : 0u;
      f32x16 z = {};
      z = mfma32(wp0, b0, z);
      return mfma32(wp1, b1, z);
    };
    const f32x16 pq = phi(aq), pk = phi(ak);
#pragma unroll
    for (int i = 0; i < 5; ++i) *(LAS unsigned*)(uintptr_t)(L0 + PQ_OFF + i * 2048 + tid * 4) = cvtpk(pq[2 * i], pq[2 * i + 1]);
    float pv[10];
#pragma unroll
    for (int i = 0; i < 5; ++i) { const unsigned u = cvtpk(pk[2 * i], pk[2 * i + 1]); pv[2 * i] = bf_lo(u); pv[2 * i + 1] = bf_hi(u); }
    u32x4 pos, neg; u32x2 ext;
    hedgehog(pv, h, pos, neg, ext);
    BAR();
    const unsigned R = L0 + 5 * SLOT + w * 3840, tk = l & 31;
    lds_wr64(R + tk * 64 + 8 * h, u32x2{pos[0], pos[1]});
    lds_wr64(R + tk * 64 + 16 + 8 * h, u32x2{pos[2], pos[3]});
    lds_wr64(R + tk * 64 + 32 + 8 * h, u32x2{neg[0], neg[1]});
    lds_wr64(R + tk * 64 + 48 + 8 * h, u32x2{neg[2], neg[3]});
    lds_wr64(R + 2048 + l * 8, ext);
    asm volatile("" ::: "memory");
    f32x16 kv0 = {}, kv1 = {};
#pragma unroll
    for (int s = 0; s < 2; ++s) {
      u32x4 vb;
#pragma unroll
      for (int i = 0; i < 4; ++i) vb[i] = (tk == 20) ? 0x3f803f80u : cvtpk(av[8 * s + 2 * i], av[8 * s + 2 * i + 1]);
      const unsigned r0 = 16 * s + 4 * h + q4;
      kv0 = mfma32(trfrag(R + r0 * 64 + 32 * g16 + 8 * pp, R + (r0 + 8) * 64 + 32 * g16 + 8 * pp), vb, kv0);
      kv1 = mfma32(trfrag(R + 2048 + r0 * 8, R + 2048 + (r0 + 8) * 8), vb, kv1);
    }
    asm volatile("" ::: "memory");
    if (tk < 24) {
      const unsigned Rl = R + (h * 24 + tk) * 16;
#pragma unroll
      for (int qd = 0; qd < 4; ++qd)
        lds_wr128(Rl + qd * 768, __builtin_bit_cast(u32x4, f32x4{kv0[4 * qd], kv0[4 * qd + 1], kv0[4 * qd + 2], kv0[4 * qd + 3]}));
      lds_wr128(Rl + 3072, __builtin_bit_cast(u32x4, f32x4{kv1[0], kv1[1], kv1[2], kv1[3]}));
    }
    WAITL0(); WAITV(9); BAR();
    if (tid < 480) {
      const int pq_ = tid >= 240, rem = tid - 240 * pq_;
      f32x4 sum = {};
#pragma unroll
      for (int e = 0; e < 4; ++e) sum += __builtin_bit_cast(f32x4, lds_rd128(L0 + 5 * SLOT + (2 * e + pq_) * 3840 + rem * 16));
      __builtin_amdgcn_raw_buffer_store_b128(__builtin_bit_cast(u32x4, sum),
          __builtin_amdgcn_make_buffer_rsrc(ws + WS_KVP, 0, 32 * 16 * 3840, 0x00020000),
          ((2 * j + pq_) * 16 + t) * 3840 + rem * 16, 0, 16);
    }
    WAITL0();
  }

  ST(3);
  u32x4 H[18];
  {
    u32x4 a[NA], hh[9];
    auto act = [&](const f32x16& c) {
      u32x4 r;
#pragma unroll
      for (int i = 0; i < 4; ++i) r[i] = cvtpk(silu_f(c[2 * i]) * c[8 + 2 * i], silu_f(c[2 * i + 1]) * c[9 + 2 * i]);
      return r;
    };
    const unsigned rA2 = L0 + h * 2048 + g16 * 1024 + q4 * 128 + (((2 * p) ^ ((q4 >> 1) + 2 * g16)) * 32) + pp * 8;
    const auto kvp = __builtin_amdgcn_make_buffer_rsrc(ws + WS_KVP, 0, 32 * 16 * 3840, 0x00020000);
    const auto kvf = __builtin_amdgcn_make_buffer_rsrc(ws + WS_KVF + j * 4096, 0, 4096, 0x00020000);
#pragma unroll
    for (int e = 0; e < NA; ++e) { const unsigned ad = (rA2 ^ ((e & 1) * 32)) + (e >> 1) * 4096; a[e] = trfrag(ad, ad + 512); }
    f32x16 c0 = {}, c1 = {};
    unsigned pk[16];
    float e0, e1, u0, u1;
    auto opf = [](float x) { asm volatile("" : "+v"(x)); return x; };
    auto defer = [&](auto nn, auto vv) {
      constexpr int n = decltype(nn)::v - 2, d = n / 4, gi = (d / 4) * 8 + d % 4, v = decltype(vv)::v;
      if constexpr (n >= 0 && n < 32) {
        if (n % 4 == 0) { e0 = opf(__builtin_amdgcn_exp2f(-LOG2E * bf_lo(pk[gi]))); e1 = opf(-LOG2E * bf_hi(pk[gi])); }
        if (n % 4 == 1) { e1 = opf(1.0f + __builtin_amdgcn_exp2f(e1)); e0 = opf(__builtin_amdgcn_rcpf(1.0f + e0)); }
        if (n % 4 == 2) { e1 = opf(__builtin_amdgcn_rcpf(e1)); u0 = opf(bf_lo(pk[gi]) * bf_lo(pk[gi + 4])); }
        if (n % 4 == 3) { u1 = bf_hi(pk[gi]) * bf_hi(pk[gi + 4]); hh[2 * v + d / 4][d % 4] = opq(cvtpk(u0 * e0, u1 * e1)); }
      }
    };
    auto dunit = [&](auto vv) {
      constexpr int v = decltype(vv)::v;
      constexpr bool last = v == 3;
      unsigned rb[2][2], seen = 0;
      u32x4 kl = {};
      const unsigned sv = sA2 + 128 * v;
      rb[0][0] = opq(rA2); rb[0][1] = opq(rA2 ^ 32);
      sfor<0, 72>([&](auto nn) {
        constexpr int n = decltype(nn)::v, i = n / 12, r = n % 12;
        if (r == 0) {
          if (!last && ((i == 1 && v == 0) || (i == 0 && v == 2))) WAITV(0); else WAITV(9);
          BAR();
          if (!last) {
            if (i == 1 && v == 0 && tid == 0) __hip_atomic_fetch_add(cnt1, 1u, __ATOMIC_RELAXED, __HIP_MEMORY_SCOPE_AGENT);
            if (i == 0 && v == 2 && tid == 0) __hip_atomic_fetch_add(cnt2, 1u, __ATOMIC_RELAXED, __HIP_MEMORY_SCOPE_AGENT);
            if (v == 1) {
              if (i == 0) seen = __hip_atomic_load(cnt1, __ATOMIC_RELAXED, __HIP_MEMORY_SCOPE_AGENT);
              if (i == 2) {
                spin16(cnt1, seen);
                const int I = 30 * t + min(tid >> 4, 29), pq_ = I >= 240;
                kl = __builtin_amdgcn_raw_buffer_load_b128(kvp, ((2 * j + pq_) * 16 + (tid & 15)) * 3840 + (I - 240 * pq_) * 16, 0, 16);
              }
              if (i == 4) {
                f32x4 sm = __builtin_bit_cast(f32x4, kl);
#pragma unroll
                for (int m = 1; m < 16; m <<= 1)
#pragma unroll
                  for (int e = 0; e < 4; ++e) sm[e] += __shfl_xor(sm[e], m);
                if (timeout) sm[0] = __uint_as_float(0x7fc00000u);
                if ((tid & 15) == 0 && tid < 480)
                  __builtin_amdgcn_raw_buffer_store_b64(u32x2{cvtpk(sm[0], sm[1]), cvtpk(sm[2], sm[3])}, kvf, (30 * t + (tid >> 4)) * 8, 0, 16);
              }
            }
          }
          if (i < 5) { rb[(i + 1) & 1][0] = opq(rA2 + (i + 1) * SLOT); rb[(i + 1) & 1][1] = opq((rA2 ^ 32) + (i + 1) * SLOT); }
          else { rb[0][0] = opq(rA2); rb[0][1] = opq(rA2 ^ 32); }
          SCHED();
        }
        if (r % 2 == 1 && grp == (r % 4 == 3)) {
          constexpr int m = r / 4;
          if (i == 0) dma16i<5 * SLOT + m * 8192>(wl, vA2, dA, sv + (480 + 32 * m) * 9216);
          else if (!last) dma16i<(i - 1) * SLOT + m * 8192>(wl, vA2, dA, sv + 128 + (96 * (i - 1) + 32 * m) * 9216);
          else if (i < 4) pieceA1(i - 1, m);
          else dma16i<(i - 1) * SLOT + m * 8192>(wl, vB, dB, sB[(i == 5) * 3 + m]);
        }
        if (n < 2) { const f32x16 z = {}; if (n == 0) c0 = mfma32(a[0], X[0], z); else c1 = mfma32(a[1], X[0], z); }
        else if (n % 2 == 0) c0 = mfma32(a[n % NA], X[n / 2], c0);
        else c1 = mfma32(a[n % NA], X[n / 2], c1);
        constexpr int q = n + NA;
        const unsigned ad = rb[(q / 12) & 1][q & 1] + ((q % 12) >> 1) * 4096;
        if (!(last && q >= 72)) a[n % NA] = trfrag(ad, ad + 512);
        if constexpr (v > 0) defer(nn, IC<v - 1>{});
        SCHED();
      });
#pragma unroll
      for (int e = 0; e < 8; ++e) { pk[e] = opq(cvtpk(c0[2 * e], c0[2 * e + 1])); pk[8 + e] = opq(cvtpk(c1[2 * e], c1[2 * e + 1])); }
      SCHED();
    };
    dunit(IC<0>{}); dunit(IC<1>{}); dunit(IC<2>{});
    l = opq(__builtin_amdgcn_mbcnt_hi(~0u, __builtin_amdgcn_mbcnt_lo(~0u, 0u)));
    vA1 = (l >> 2) * 9216 + ((((l >> 1) & 1) ^ p) * 32) + (l & 1) * 16;
    vB = (8 * p + 4 * (l >> 5) + ((l >> 2) & 3)) * 1152 + 64 * ((l >> 4) & 1) + 16 * (l & 3);
    dunit(IC<3>{});
    l = opq(__builtin_amdgcn_mbcnt_hi(~0u, __builtin_amdgcn_mbcnt_lo(~0u, 0u)));
    h = l >> 5; g16 = (l >> 4) & 1; q4 = (l >> 2) & 3; pp = l & 3; l16 = l * 16;
    const unsigned rA1 = L0 + g16 * 1024 + h * 512 + q4 * 64 + (p ^ g16) * 32 + pp * 8;
    u32x2 kc[5];
    {
      const unsigned r0 = opq(rA1), r2 = opq(rA1 + 2 * SLOT);
      unsigned seen = __hip_atomic_load(cnt2, __ATOMIC_RELAXED, __HIP_MEMORY_SCOPE_AGENT);
      c0 = f32x16{};
      sfor<0, 36>([&](auto nn) {
        constexpr int s = decltype(nn)::v;
        if (s == 0) {
#pragma unroll
          for (int e = 0; e < NA; ++e) a[e] = trfrag(r0 + e * 2048, r0 + e * 2048 + 256);
          WAITV(9); BAR();
        }
        if (s == 12) {
          WAITV(9); BAR();
          spin16(cnt2, seen);
          const unsigned ka = (p * 240 + h * 24 + min(l & 31, 23)) * 8;
#pragma unroll
          for (int e = 0; e < 5; ++e) kc[e] = __builtin_amdgcn_raw_buffer_load_b64(kvf, ka + e * 384, 0, 16);
          SCHED();
        }
        if (s < 12 && s % 2 == 1 && grp == (s % 4 == 3)) dma16i<5 * SLOT + (s / 4) * 8192>(wl, vB, dB, sB[6 + (s < 12 ? s / 4 : 0)]);
        c0 = mfma32(a[s % NA], X[s], c0);
        constexpr int q = s + NA;
        if (q < 36) { const unsigned ad = q < 24 ? r0 + q * 2048 : r2 + (q - 24) * 2048; a[s % NA] = trfrag(ad, ad + 256); }
        defer(nn, IC<3>{});
        SCHED();
      });
      hh[8] = act(c0);
    }
    ST(4);
    BAR();
    ST(5);
#pragma unroll
    for (int e = 0; e < 8; ++e) lds_wr128(L0 + (tg4 * 18 + 2 * jo + 4 * (e >> 1) + 2 * p + (e & 1)) * 1024 + l16, hh[e]);
    lds_wr128(L0 + (tg4 * 18 + 16 * (1 - jo) + p) * 1024 + l16, hh[8]);
    WAITL0(); BAR();
#pragma unroll
    for (int s = 0; s < 18; ++s) H[s] = lds_rd128(L0 + tg4 * 18432 + l16 + s * 1024);
    const u32x4 k0 = {kc[0][0], kc[0][1], kc[1][0], kc[1][1]}, k1 = {kc[2][0], kc[2][1], kc[3][0], kc[3][1]};
    const u32x4 k2 = {h ? 0u : kc[4][0], h ? 0u : kc[4][1], 0u, 0u};
    WAITL0();
    WAITV(0); BAR();
    sfor<0, 9>([&](auto mm) { constexpr int m = decltype(mm)::v; dma16i<(m / 3) * SLOT + (m % 3) * 8192>(wl, vB, dB, sB[m] + 256); });
    ST(6);
    float pv[10];
#pragma unroll
    for (int i = 0; i < 5; ++i) {
      const unsigned u = *(LAS unsigned*)(uintptr_t)(L0 + PQ_OFF + i * 2048 + w * 256 + l * 4);
      pv[2 * i] = bf_lo(u); pv[2 * i + 1] = bf_hi(u);
    }
    u32x4 pos, neg; u32x2 ext;
    hedgehog(pv, h, pos, neg, ext);
    f32x16 on = {};
    on = mfma32(k0, pos, on); on = mfma32(k1, neg, on); on = mfma32(k2, u32x4{ext[0], ext[1], 0u, 0u}, on);
    const float inv = timeout ? __uint_as_float(0x7fc00000u) : __builtin_amdgcn_rcpf(xhalf_sum(h ? on[8] : 0.f) + 1e-6f);
    char* op = ws + WS_O + (size_t)(32 * tg + (l & 31)) * 1152 + (36 * j + 16 * p + 4 * h) * 2;
    const u32x2 o0 = {cvtpk(on[0] * inv, on[1] * inv), cvtpk(on[2] * inv, on[3] * inv)};
    const u32x2 o1 = {cvtpk(on[4] * inv, on[5] * inv), cvtpk(on[6] * inv, on[7] * inv)};
    const u32x2 o2 = {cvtpk(on[8] * inv, on[9] * inv), cvtpk(on[10] * inv, on[11] * inv)};
    if (p && !h) *(unsigned*)(op + 4) = o0[1]; else *(u32x2*)op = o0;
    *(u32x2*)(op + 16) = o1;
    if (!h) { if (p) *(u32x2*)(op + 32) = o2; else *(unsigned*)(op + 32) = o2[0]; }
  }

  ST(7);
  {
    unsigned cur = L0 + HALF, oth = L0;
    const unsigned rB = h * 512 + p * 256 + q4 * 64 + g16 * 32 + pp * 8;
    const auto yr = __builtin_amdgcn_make_buffer_rsrc(ws + WS_YP, 0, (unsigned)YP_BYTES, 0x00020000);
    const unsigned ybase = ((j * 64 + tg) * 36 + 2 * p) * 1024;
    u32x4 a[NB];
#pragma unroll
    for (int e = 0; e < NB; ++e) a[e] = trfrag(cur + rB + e * 2048, cur + rB + e * 2048 + 1024);
    auto period = [&](auto last_, int P) {
      constexpr bool last = decltype(last_)::v;
      const unsigned rc = opq(cur + rB), rc2 = opq(cur + rB + 2 * SLOT), rn = opq(oth + rB);
      const unsigned wc = cur + w * 1024, wo_ = oth + w * 1024, sp = 256 * P;
      f32x16 acc = {};
      sfor<0, (last ? 18 : 36)>([&](auto nn) {
        constexpr int n = decltype(nn)::v, i = n / 12, r = n % 12;
        if (r == 0) {
          if (last) { if (i == 0) { WAITV(6); BAR(); } }
          else if (P == 3) { if (i == 0) WAITV(13); else if (i == 1) WAITV(10); else WAITV(7); BAR(); }
          else if (i > 0 || P > 0) { WAITV(13); BAR(); }
          SCHED();
        }
        if (!last && r % 2 == 1 && grp == (r % 4 == 3) && P < 3) {
          constexpr int m = r / 4;
          if (i == 0) { if (P > 0) dma16i<2 * SLOT + m * 8192>(wo_, vB, dB, sB[6 + m] + 256 + sp); }
          else dma16i<(i ? i - 1 : 0) * SLOT + m * 8192>(wc, vB, dB, sB[(i ? i - 1 : 0) * 3 + m] + 512 + sp);
        }
        if (n % 18 == 0) { const f32x16 z = {}; acc = mfma32(a[n % NB], H[0], z); }
        else acc = mfma32(a[n % NB], H[n % 18], acc);
        constexpr int q = n + NB;
        const unsigned ad = q < 24 ? rc + q * 2048 : q < 36 ? rc2 + (q - 24) * 2048 : rn + (q - 36) * 2048;
        if (!(last && q >= 18)) a[n % NB] = trfrag(ad, ad + 1024);
        SCHED();
        if (n % 18 == 17) {
          const unsigned so = ybase + (2 * P + n / 18) * 4096;
#pragma unroll
          for (int e = 0; e < 2; ++e) {
            const auto s0 = __builtin_amdgcn_permlane16_swap(cvtpk(acc[8 * e], acc[8 * e + 1]), cvtpk(acc[8 * e + 4], acc[8 * e + 5]), false, false);
            const auto s1 = __builtin_amdgcn_permlane16_swap(cvtpk(acc[8 * e + 2], acc[8 * e + 3]), cvtpk(acc[8 * e + 6], acc[8 * e + 7]), false, false);
            const u32x4 y = {s0[0], s1[0], s0[1], s1[1]};
            __builtin_amdgcn_raw_buffer_store_b128(y, yr, l16 + e * 1024, so, YAUX);
          }
          SCHED();
        }
      });
      const unsigned tmp = cur; cur = oth; oth = tmp;
    };
#pragma unroll 1
    for (int P = 0; P < 4; ++P) period(IC<0>{}, P);
    period(IC<1>{}, 4);
  }
  ST(8);
  ST(9); ST(13); STR(14);
}

__global__ __launch_bounds__(576, 3) void k_final(const unsigned short* __restrict__ x, const char* __restrict__ ws,
                                                  unsigned short* __restrict__ out) {
  __shared__ __attribute__((aligned(1024))) char lds[36864];
  const unsigned L0 = (unsigned)(uintptr_t)(LAS char*)lds;
  const int b = blockIdx.x, tg = (b & 7) * 8 + (b >> 5), dq = (b >> 3) & 3;
  const int w = __builtin_amdgcn_readfirstlane(threadIdx.x >> 6), l = threadIdx.x & 63, g = l >> 4, hb = 9 * dq + w;
  const i32x4 dO = mkdesc(ws + WS_O, S_ * D_ * 2);
#pragma unroll
  for (int m = 0; m < 4; ++m) {
    const int fi = 4 * w + m, tb = fi >= 18, s = fi - 18 * tb;
    dma16(L0 + fi * 1024, (l & 15) * 1152 + g * 16, dO, (32 * tg + 16 * tb) * 1152 + 64 * s);
  }
  u32x4 wf[18];
  const u32x4* wsrc = (const u32x4*)(ws + WS_WOT + (size_t)hb * 18432) + l;
#pragma unroll
  for (int s = 0; s < 18; ++s) wf[s] = wsrc[s * 64];
  const size_t xo = (size_t)(32 * tg + (l & 15)) * D_ + 16 * hb + 8 * (g & 1) + 4 * (g >> 1);
  const u32x2 x0 = *(const u32x2*)(x + xo), x1 = *(const u32x2*)(x + xo + 16 * D_);
  u32x4 yp[16];
  const auto yr = __builtin_amdgcn_make_buffer_rsrc((void*)(ws + WS_YP), 0, (unsigned)YP_BYTES, 0x00020000);
#pragma unroll
  for (int jj = 0; jj < 16; ++jj) yp[jj] = __builtin_amdgcn_raw_buffer_load_b128(yr, l * 16, ((jj * 64 + tg) * 36 + hb) * 1024, YLD);
  WAITV(36); BAR();
  f32x4 s0 = {}, s1 = {};
#pragma unroll
  for (int s = 0; s < 18; ++s) {
    s0 = mfma16(wf[s], lds_rd128(L0 + s * 1024 + l * 16), s0);
    s1 = mfma16(wf[s], lds_rd128(L0 + (18 + s) * 1024 + l * 16), s1);
  }
  f32x4 y0 = {}, y1 = {};
#pragma unroll
  for (int jj = 0; jj < 16; ++jj) {
    y0 += f32x4{bf_lo(yp[jj][0]), bf_hi(yp[jj][0]), bf_lo(yp[jj][1]), bf_hi(yp[jj][1])};
    y1 += f32x4{bf_lo(yp[jj][2]), bf_hi(yp[jj][2]), bf_lo(yp[jj][3]), bf_hi(yp[jj][3])};
  }
  const u32x2 r0 = {cvtpk(bf_lo(x0[0]) + s0[0] * silu_f(y0[0]), bf_hi(x0[0]) + s0[1] * silu_f(y0[1])),
                    cvtpk(bf_lo(x0[1]) + s0[2] * silu_f(y0[2]), bf_hi(x0[1]) + s0[3] * silu_f(y0[3]))};
  const u32x2 r1 = {cvtpk(bf_lo(x1[0]) + s1[0] * silu_f(y1[0]), bf_hi(x1[0]) + s1[1] * silu_f(y1[1])),
                    cvtpk(bf_lo(x1[1]) + s1[2] * silu_f(y1[2]), bf_hi(x1[1]) + s1[3] * silu_f(y1[3]))};
  *(u32x2*)(out + xo) = r0;
  *(u32x2*)(out + xo + 16 * D_) = r1;
}

extern "C" void kernel_launch(void* const* d_in, const int* in_sizes, int n_in, void* d_out, int out_size, void* d_ws,
                              size_t ws_size, hipStream_t stream) {
  const unsigned short* x = (const unsigned short*)d_in[0];
  const unsigned short* wqkv = (const unsigned short*)d_in[1];
  const unsigned short* wphi = (const unsigned short*)d_in[2];
  const unsigned short* wo = (const unsigned short*)d_in[3];
  const unsigned short* wg = (const unsigned short*)d_in[5];
  const unsigned short* wu = (const unsigned short*)d_in[6];
  const unsigned short* wd = (const unsigned short*)d_in[7];
  char* ws = (char*)d_ws;
  k_prep<<<256, 512, 0, stream>>>(x, wqkv, wo, wphi, ws);
  k_main<<<256, 512, 0, stream>>>(wg, wu, wd, ws);
  k_final<<<256, 576, 0, stream>>>(x, ws, (unsigned short*)d_out);
}
```

```cpp
#include <hip/hip_runtime.h>
#include <hip/hip_bf16.h>
#include <stdint.h>

typedef __attribute__((ext_vector_type(8))) short s16x8;
typedef __attribute__((ext_vector_type(4))) short s16x4;
typedef __attribute__((ext_vector_type(8))) __bf16 bf16x8;
typedef __attribute__((ext_vector_type(2))) __bf16 bf16x2;
typedef __attribute__((ext_vector_type(16))) float f32x16;
typedef __attribute__((ext_vector_type(4))) float f32x4;
typedef __attribute__((ext_vector_type(2))) float f32x2;
typedef __attribute__((ext_vector_type(4))) unsigned u32x4;
typedef __attribute__((ext_vector_type(2))) unsigned u32x2;
typedef __attribute__((ext_vector_type(4))) int i32x4;
#define LAS __attribute__((address_space(3)))
#define GAS __attribute__((address_space(1)))
#define DEV __device__ __forceinline__

template <int I> struct IC { static constexpr int v = I; };
template <int B, int E, class Fn> DEV void sfor(Fn&& fn) {
  if constexpr (B < E) { fn(IC<B>{}); sfor<B + 1, E>(fn); }
}

constexpr int S_ = 2048, D_ = 576, F_ = 4608;
constexpr size_t WS_XN = 0;
constexpr size_t WS_WOT = WS_XN + 64 * 36 * 1024;
constexpr size_t WS_KVP = WS_WOT + 36 * 18 * 1024;
constexpr size_t WS_O = WS_KVP + 32 * 16 * 3840;
constexpr size_t WS_CNT = WS_O + 2048 * 576 * 2;
constexpr size_t WS_KVF = WS_CNT + 32 * 256;
constexpr size_t WS_QI = WS_KVF + 16 * 4096;
constexpr size_t WS_YP = WS_QI + 16 * 6 * 24576;
constexpr size_t YP_BYTES = (size_t)16 * 64 * 36 * 1024;

DEV unsigned cvtpk(float lo, float hi) {
  f32x2 v = {lo, hi};
  return __builtin_bit_cast(unsigned, __builtin_convertvector(v, bf16x2));
}
DEV float bf_lo(unsigned u) { return __uint_as_float(u << 16); }
DEV float bf_hi(unsigned u) { return __uint_as_float(u & 0xffff0000u); }
DEV f32x16 mfma32(u32x4 a, u32x4 b, f32x16 c) {
  return __builtin_amdgcn_mfma_f32_32x32x16_bf16(__builtin_bit_cast(bf16x8, a), __builtin_bit_cast(bf16x8, b), c, 0, 0, 0);
}
DEV f32x4 mfma16(u32x4 a, u32x4 b, f32x4 c) {
  return __builtin_amdgcn_mfma_f32_16x16x32_bf16(__builtin_bit_cast(bf16x8, a), __builtin_bit_cast(bf16x8, b), c, 0, 0, 0);
}
DEV u32x2 trd(unsigned addr) {
  return __builtin_bit_cast(u32x2, __builtin_amdgcn_ds_read_tr16_b64_v4i16((LAS s16x4*)(uintptr_t)addr));
}
DEV u32x4 trfrag(unsigned a0, unsigned a1) {
  u32x2 lo = trd(a0), hi = trd(a1);
  u32x4 r = {lo[0], lo[1], hi[0], hi[1]};
  return r;
}
DEV u32x4 lds_rd128(unsigned a) { return *(LAS u32x4*)(uintptr_t)a; }
DEV u32x2 lds_rd64(unsigned a) { return *(LAS u32x2*)(uintptr_t)a; }
DEV void lds_wr128(unsigned a, u32x4 v) { *(LAS u32x4*)(uintptr_t)a = v; }
DEV void lds_wr64(unsigned a, u32x2 v) { *(LAS u32x2*)(uintptr_t)a = v; }
DEV i32x4 mkdesc(const void* p, unsigned bytes) {
  uint64_t a = (uint64_t)p;
  i32x4 d = {(int)__builtin_amdgcn_readfirstlane((unsigned)a), (int)__builtin_amdgcn_readfirstlane((unsigned)(a >> 32) & 0xffffu),
             (int)bytes, 0x00020000};
  return d;
}
DEV void dma16(unsigned lds, unsigned voff, i32x4 desc, unsigned soff) {
  asm volatile("s_mov_b32 m0, %0\n\ts_nop 0\n\tbuffer_load_dwordx4 %1, %2, %3 offen lds"
               :: "s"(lds), "v"(voff), "s"(desc), "s"(soff) : "memory");
}
template <unsigned OFF> DEV void dma16i(unsigned lds, unsigned voff, i32x4 desc, unsigned soff) {
  asm volatile("s_add_u32 m0, %0, %4\n\ts_nop 0\n\tbuffer_load_dwordx4 %1, %2, %3 offen lds"
               :: "s"(lds), "v"(voff), "s"(desc), "s"(soff), "i"(OFF) : "memory", "scc");
}
#define WAITV(n) asm volatile("s_waitcnt vmcnt(" #n ")" ::: "memory")
#define ST(k)
#define STR(k)
#define SCHED() __builtin_amdgcn_sched_barrier(0)
DEV unsigned opq(unsigned v) { asm volatile("" : "+v"(v)); return v; }
#define WAITL0() asm volatile("s_waitcnt lgkmcnt(0)" ::: "memory")
#define BAR() do { asm volatile("" ::: "memory"); __builtin_amdgcn_s_barrier(); asm volatile("" ::: "memory"); } while (0)
DEV float xhalf_max(float x) {
  auto t = __builtin_amdgcn_permlane32_swap(__float_as_uint(x), __float_as_uint(x), false, false);
  return fmaxf(__uint_as_float(t[0]), __uint_as_float(t[1]));
}
DEV float xhalf_sum(float x) {
  auto t = __builtin_amdgcn_permlane32_swap(__float_as_uint(x), __float_as_uint(x), false, false);
  return __uint_as_float(t[0]) + __uint_as_float(t[1]);
}
constexpr float LOG2E = 1.4426950408889634f;
DEV float silu_f(float g) { return g * __builtin_amdgcn_rcpf(1.0f + __builtin_amdgcn_exp2f(-LOG2E * g)); }

DEV void hedgehog(const float (&pv)[10], int h, u32x4& pos, u32x4& neg, u32x2& ext) {
  float mx = pv[0], mn = pv[0];
#pragma unroll
  for (int r = 1; r < 8; ++r) { mx = fmaxf(mx, pv[r]); mn = fminf(mn, pv[r]); }
  if (h == 0) { mx = fmaxf(mx, fmaxf(pv[8], pv[9])); mn = fminf(mn, fminf(pv[8], pv[9])); }
  mx = xhalf_max(mx); mn = -xhalf_max(-mn);
  float ep[10], en[10], sp = 0.f, sn = 0.f;
#pragma unroll
  for (int r = 0; r < 10; ++r) {
    ep[r] = __builtin_amdgcn_exp2f((pv[r] - mx) * LOG2E);
    en[r] = __builtin_amdgcn_exp2f((mn - pv[r]) * LOG2E);
    if (r >= 8) { ep[r] = h ? 0.f : ep[r]; en[r] = h ? 0.f : en[r]; }
    sp += ep[r]; sn += en[r];
  }
  float ip = __builtin_amdgcn_rcpf(xhalf_sum(sp)), in = __builtin_amdgcn_rcpf(xhalf_sum(sn));
#pragma unroll
  for (int i = 0; i < 4; ++i) {
    pos[i] = cvtpk(ep[2 * i] * ip, ep[2 * i + 1] * ip);
    neg[i] = cvtpk(en[2 * i] * in, en[2 * i + 1] * in);
  }
  ext[0] = cvtpk(ep[8] * ip, ep[9] * ip);
  ext[1] = cvtpk(en[8] * in, en[9] * in);
}

__global__ __launch_bounds__(512) void k_prep(const unsigned short* __restrict__ x, const unsigned short* __restrict__ wqkv,
                                              const unsigned short* __restrict__ wo,
                                              const unsigned short* __restrict__ wphi, char* __restrict__ ws) {
  const int b = blockIdx.x, w = threadIdx.x >> 6, l = threadIdx.x & 63;
  if (w < 2) {
    const int tok = 8 * b + 4 * w + (l >> 4), i = l & 15;
    u32x2 v[9];
#pragma unroll
    for (int u = 0; u < 9; ++u) v[u] = *(const u32x2*)(x + (size_t)tok * D_ + 4 * (i + 16 * u));
    float f[36], sum = 0.f;
#pragma unroll
    for (int u = 0; u < 9; ++u) {
      f[4 * u] = bf_lo(v[u][0]); f[4 * u + 1] = bf_hi(v[u][0]); f[4 * u + 2] = bf_lo(v[u][1]); f[4 * u + 3] = bf_hi(v[u][1]);
      sum += (f[4 * u] + f[4 * u + 1]) + (f[4 * u + 2] + f[4 * u + 3]);
    }
#pragma unroll
    for (int m = 1; m < 16; m <<= 1) sum += __shfl_xor(sum, m);
    const float mean = sum * (1.0f / D_);
    float sq = 0.f;
#pragma unroll
    for (int e = 0; e < 36; ++e) { f[e] -= mean; sq += f[e] * f[e]; }
#pragma unroll
    for (int m = 1; m < 16; m <<= 1) sq += __shfl_xor(sq, m);
    const float rs = 1.0f / sqrtf(sq * (1.0f / D_) + 1e-5f);
    char* dst = ws + WS_XN + (size_t)(tok >> 5) * 36864 + (tok & 31) * 16;
#pragma unroll
    for (int u = 0; u < 9; ++u) {
      const int c8 = i + 16 * u, c = c8 >> 1;
      u32x2 o = {cvtpk(f[4 * u] * rs, f[4 * u + 1] * rs), cvtpk(f[4 * u + 2] * rs, f[4 * u + 3] * rs)};
      *(u32x2*)(dst + (c >> 1) * 1024 + (c & 1) * 512 + (c8 & 1) * 8) = o;
    }
  } else if (w >= 5) {
    u32x4 v[3];
#pragma unroll
    for (int m = 0; m < 3; ++m) {
      const int id = 576 * b + (int)threadIdx.x - 320 + 192 * m, jj = id / 9216, g = (id % 9216) / 1536;
      const int c = min(id % 1536, 1439), k16 = c / 240, r = c % 240, seg = r / 80, pos = (r % 80) / 5, ch = r % 5;
      const int row = 96 * g + 16 * k16 + 8 * ((pos >> 1) & 1) + 4 * (pos & 1) + (pos >> 2);
      const int off = min(row * 3456 + seg * 1152 + ((72 * jj) & ~15) + 16 * ch, D_ * 3 * D_ * 2 - 16);
      v[m] = *(const u32x4*)((const char*)wqkv + off);
    }
#pragma unroll
    for (int m = 0; m < 3; ++m) {
      const int id = 576 * b + (int)threadIdx.x - 320 + 192 * m, g = (id % 9216) / 1536;
      if (!(id % 1536 >= 1440 && (g == 2 || g == 3))) *(u32x4*)(ws + WS_QI + (size_t)id * 16) = v[m];
    }
  } else {
    for (int fi = 3 * b + (w - 2); fi < 648 + 64; fi += 768) {
      unsigned short e[8];
      char* dst;
      if (fi < 648) {
        const int hb = fi / 18, s = fi % 18, g = l >> 4, d = 16 * hb + (l & 15);
#pragma unroll
        for (int jj = 0; jj < 8; ++jj) e[jj] = wo[(size_t)(32 * s + 8 * g + jj) * D_ + d];
        dst = ws + WS_WOT + (size_t)fi * 1024;
      } else {
        const int q = fi - 648, head = q >> 1, s = q & 1, f = l & 31, h = l >> 5;
#pragma unroll
        for (int jj = 0; jj < 8; ++jj) {
          const int d = 16 * s + 8 * (jj >> 2) + 4 * h + (jj & 3) - 2 * (head & 1);
          const bool ok = f < 18 && d >= 0 && d < 18;
          const unsigned short t = wphi[head * 324 + (ok ? d * 18 + f : 0)];
          e[jj] = ok ? t : (unsigned short)0;
        }
        dst = ws + WS_QI + (size_t)((head >> 1) * 6 + 2 + (head & 1)) * 24576 + 23040;
        if (s) {
          *(u32x2*)(dst + 1024 + l * 8) = u32x2{e[0] | ((unsigned)e[1] << 16), e[2] | ((unsigned)e[3] << 16)};
          continue;
        }
      }
      u32x4 o = {e[0] | ((unsigned)e[1] << 16), e[2] | ((unsigned)e[3] << 16), e[4] | ((unsigned)e[5] << 16), e[6] | ((unsigned)e[7] << 16)};
      *(u32x4*)(dst + l * 16) = o;
    }
  }
  if (b == 0 && threadIdx.x < 32) *(unsigned*)(ws + WS_CNT + threadIdx.x * 256) = 0u;
}

constexpr unsigned SLOT = 24576, HALF = 3 * SLOT, SPARE = 6 * SLOT, LDS_MAIN = 163840;
#define JMAP (4 * (b & 3) + ((b >> 3) & 3))
#define TMAP (((b >> 2) & 1) * 8 + (b >> 5))
#define RY 1
#define RO 8
#define NA 3
#define NB 6
#define NQ 6
#define YH 0
#define YLD 2
#define YAUX 16

__global__ __launch_bounds__(512, 2) void k_main(const unsigned short* __restrict__ wg, const unsigned short* __restrict__ wu,
                                                 const unsigned short* __restrict__ wd, char* __restrict__ ws) {
  __shared__ __attribute__((aligned(1024))) char lds[LDS_MAIN];
  const unsigned L0 = (unsigned)(uintptr_t)(LAS char*)lds;
  const int b = blockIdx.x, tid = threadIdx.x;
  const int j = JMAP, t = TMAP, jo = j & 1;
  const int w = __builtin_amdgcn_readfirstlane(tid >> 6), tg4 = w >> 1, p = w & 1;
  int l = tid & 63, h = l >> 5, g16 = (l >> 4) & 1, q4 = (l >> 2) & 3, pp = l & 3;
  const int tg = 4 * t + tg4;
  GAS unsigned* const cnt1 = (GAS unsigned*)(ws + WS_CNT + j * 256);
  GAS unsigned* const cnt2 = cnt1 + 1024;

  const i32x4 dW = mkdesc(ws, (unsigned)WS_YP), dA = mkdesc(p ? wu : wg, D_ * F_ * 2), dB = mkdesc(wd, D_ * F_ * 2);
  unsigned l16 = l * 16;
  const unsigned vA2 = (l >> 3) * 9216 + ((((l >> 1) & 3) ^ (((l >> 4) & 1) + 2 * (w >> 2))) * 32) + (l & 1) * 16;
  unsigned vA1 = 0, vB = 0;
  auto pieceX = [&](int half, int m) {
    const int n = 9 * w + m, tq = w >> 1, s = 18 * half + n - 18 * tq;
    dma16(L0 + n * 1024, l16, dW, (unsigned)WS_XN + ((4 * t + tq) * 36 + s) * 1024);
  };
  auto pieceQ = [&](int g, int m) {
    dma16(L0 + (3 + g % 3) * SLOT + (w + 8 * m) * 1024, l16, dW, (unsigned)WS_QI + (j * 6 + g) * SLOT + (w + 8 * m) * 1024);
  };
  auto pieceA2 = [&](int v, int i, int m) {
    dma16(L0 + i * SLOT + (w + 8 * m) * 1024, vA2, dA,
          (96 * i + 16 * ((w >> 2) + 2 * m) + 8 * (tg4 & 1)) * 9216 + (288 * j + 32 * jo + 64 * v) * 2);
  };
  auto pieceA1 = [&](int i, int m) {
    dma16(L0 + i * SLOT + (w + 8 * m) * 1024, vA1, dA, (192 * i + 16 * (tg4 + 4 * m)) * 9216 + (288 * j + 256 * (1 - jo)) * 2);
  };
  const bool grp = w >= 4;
  const unsigned wl = L0 + w * 1024;
  const unsigned sA2 = (16 * (w >> 2) + 8 * (tg4 & 1)) * 9216 + (288 * j + 32 * jo) * 2;
  unsigned sB[9];
#pragma unroll
  for (int e = 0; e < 9; ++e) {
    const int it = 12 * (e / 3) + tg4 + 4 * (e % 3), dp = it >= 18;
    sB[e] = (288 * j + 16 * (it - 18 * dp)) * 1152 + 128 * dp;
  }
  bool timeout = false;
  auto spin16 = [&](GAS unsigned* c, unsigned seen) {
    unsigned spins = 0;
    while (__builtin_amdgcn_readfirstlane(seen) < 16u) {
      __builtin_amdgcn_s_sleep(2);
      seen = __hip_atomic_load(c, __ATOMIC_RELAXED, __HIP_MEMORY_SCOPE_AGENT);
      if (++spins > (1u << 22)) { timeout = true; break; }
    }
  };

  STR(15); ST(0);
  u32x4 X[36], wp0, wp1;
#pragma unroll
  for (int m = 0; m < 9; ++m) pieceX(0, m);
#pragma unroll
  for (int m = 0; m < 9; ++m) pieceQ(m / 3, m % 3);
  WAITV(3); BAR();
  const unsigned rx = opq(L0 + tg4 * 18432 + l16);
#pragma unroll
  for (int s = 0; s < 18; ++s) X[s] = lds_rd128(rx + s * 1024);
  WAITL0(); BAR();
  ST(1);

  f32x16 aq = {}, ak = {}, av = {};
  {
    const unsigned rq = L0 + 3 * SLOT + (4 * q4 + 2 * h) * 80 + ((72 * j) & 15) + 32 * p + 32 * g16 + 8 * pp;
#define QADDR(n) (rb[((n) / 18) & 1] + (((n) % 18) / 3) * 3840 + ((n) % 3) * 1280)
    u32x4 a[NQ];
    unsigned rb[2] = {rq, rq};
    sfor<0, 108>([&](auto nn) {
      constexpr int n = decltype(nn)::v, g = n / 18, r = n % 18;
      if (r == 0) {
        if (g == 1 || g == 4) WAITV(9);
        if (g == 3) WAITV(3);
        if (g) BAR();
        rb[(g + 1) & 1] = opq(rq + ((g + 1) % 3) * SLOT);
        if (g == 3) {
#pragma unroll
          for (int s = 18; s < 36; ++s) X[s] = lds_rd128(rx + (s - 18) * 1024);
          const unsigned wa = L0 + (p ? 3 : 5) * SLOT + 23040;
          wp0 = lds_rd128(wa + l16);
          const u32x2 w1 = lds_rd64(wa + 1024 + l * 8);
          wp1 = u32x4{w1[0], w1[1], 0u, 0u};
        }
        if (g == 0 || g == 3) {
#pragma unroll
          for (int e = 0; e < NQ; ++e) a[e] = trfrag(QADDR(n + e), QADDR(n + e) + 80);
        }
        if (g == 3) { WAITL0(); BAR(); }
        SCHED();
      }
      if (g == 0 && r < 9) pieceX(1, r);
      if ((g == 1 || g == 2) && r < 3) pieceQ(g + 2, r);
      if (g == 3 && r < 3) pieceQ(5, r);
      if (g == 3 && r >= 3 && r < 12) pieceA2(0, (r - 3) / 3, r % 3);
      if (g >= 4 && r % 6 == 1) pieceA2(0, g - 1, r / 6);
      constexpr int s = n / 3;
      if (n % 3 == 0) aq = mfma32(a[n % NQ], X[s], aq);
      else if (n % 3 == 1) ak = mfma32(a[n % NQ], X[s], ak);
      else av = mfma32(X[s], a[n % NQ], av);
      if (n + NQ < 108 && (n + NQ < 54 || n >= 54)) a[n % NQ] = trfrag(QADDR(n + NQ), QADDR(n + NQ) + 80);
      if (n == 107 || n == 53) asm volatile("" : "+v"(aq), "+v"(ak), "+v"(av));
      SCHED();
    });
  }
  ST(2);
  constexpr unsigned PQ_OFF = 5 * SLOT + 8 * 3840;
  {
    auto phi = [&](const f32x16& c) {
      u32x4 b0 = {cvtpk(c[0], c[1]), cvtpk(c[2], c[3]), cvtpk(c[4], c[5]), cvtpk(c[6], c[7])};
      u32x4 b1 = {cvtpk(c[8], c[9]), cvtpk(c[10], c[11]), 0u, 0u};
      b0[0] = (p && !h) ? 0u : b0[0];
      b1[0] = h ? 0u : b1[0];
      b1[1] = (p && !h) ? b1[1] : 0u;
      f32x16 z = {};
      z = mfma32(wp0, b0, z);
      return mfma32(wp1, b1, z);
    };
    const f32x16 pq = phi(aq), pk = phi(ak);
#pragma unroll
    for (int i = 0; i < 5; ++i) *(LAS unsigned*)(uintptr_t)(L0 + PQ_OFF + i * 2048 + tid * 4) = cvtpk(pq[2 * i], pq[2 * i + 1]);
    float pv[10];
#pragma unroll
    for (int i = 0; i < 5; ++i) { const unsigned u = cvtpk(pk[2 * i], pk[2 * i + 1]); pv[2 * i] = bf_lo(u); pv[2 * i + 1] = bf_hi(u); }
    u32x4 pos, neg; u32x2 ext;
    hedgehog(pv, h, pos, neg, ext);
    BAR();
    const unsigned R = L0 + 5 * SLOT + w * 3840, tk = l & 31;
    lds_wr64(R + tk * 64 + 8 * h, u32x2{pos[0], pos[1]});
    lds_wr64(R + tk * 64 + 16 + 8 * h, u32x2{pos[2], pos[3]});
    lds_wr64(R + tk * 64 + 32 + 8 * h, u32x2{neg[0], neg[1]});
    lds_wr64(R + tk * 64 + 48 + 8 * h, u32x2{neg[2], neg[3]});
    lds_wr64(R + 2048 + l * 8, ext);
    asm volatile("" ::: "memory");
    f32x16 kv0 = {}, kv1 = {};
#pragma unroll
    for (int s = 0; s < 2; ++s) {
      u32x4 vb;
#pragma unroll
      for (int i = 0; i < 4; ++i) vb[i] = (tk == 20) ? 0x3f803f80u : cvtpk(av[8 * s + 2 * i], av[8 * s + 2 * i + 1]);
      const unsigned r0 = 16 * s + 4 * h + q4;
      kv0 = mfma32(trfrag(R + r0 * 64 + 32 * g16 + 8 * pp, R + (r0 + 8) * 64 + 32 * g16 + 8 * pp), vb, kv0);
      kv1 = mfma32(trfrag(R + 2048 + r0 * 8, R + 2048 + (r0 + 8) * 8), vb, kv1);
    }
    asm volatile("" ::: "memory");
    if (tk < 24) {
      const unsigned Rl = R + (h * 24 + tk) * 16;
#pragma unroll
      for (int qd = 0; qd < 4; ++qd)
        lds_wr128(Rl + qd * 768, __builtin_bit_cast(u32x4, f32x4{kv0[4 * qd], kv0[4 * qd + 1], kv0[4 * qd + 2], kv0[4 * qd + 3]}));
      lds_wr128(Rl + 3072, __builtin_bit_cast(u32x4, f32x4{kv1[0], kv1[1], kv1[2], kv1[3]}));
    }
    WAITL0(); WAITV(9); BAR();
    if (tid >= 272) {
      const int rem = tid - 272;
#pragma unroll
      for (int pq_ = 0; pq_ < 2; ++pq_) {
        f32x4 sum = {};
#pragma unroll
        for (int e = 0; e < 4; ++e) sum += __builtin_bit_cast(f32x4, lds_rd128(L0 + 5 * SLOT + (2 * e + pq_) * 3840 + rem * 16));
        __builtin_amdgcn_raw_buffer_store_b128(__builtin_bit_cast(u32x4, sum),
            __builtin_amdgcn_make_buffer_rsrc(ws + WS_KVP, 0, 32 * 16 * 3840, 0x00020000),
            ((2 * j + pq_) * 16 + t) * 3840 + rem * 16, 0, 16);
      }
    }
    WAITL0();
  }

  ST(3);
  u32x4 H[18], k0, k1, k2;
  {
    u32x4 a[NA], hh[9];
#pragma unroll
    for (int s = 0; s < 18; ++s)
#pragma unroll
      for (int i = 0; i < 4; ++i) {
        const auto sw = __builtin_amdgcn_permlane16_swap(X[2 * s][i], X[2 * s + 1][i], false, false);
        X[2 * s][i] = sw[0]; X[2 * s + 1][i] = sw[1];
      }
    const unsigned rA2 = L0 + g16 * 4096 + h * 2048 + q4 * 128 + (((2 * p) ^ ((q4 >> 1) + 2 * g16)) * 32) + pp * 8;
    const auto kvp = __builtin_amdgcn_make_buffer_rsrc(ws + WS_KVP, 0, 32 * 16 * 3840, 0x00020000);
    const auto kvf = __builtin_amdgcn_make_buffer_rsrc(ws + WS_KVF + j * 4096, 0, 4096, 0x00020000);
#pragma unroll
    for (int e = 0; e < NA; ++e) { const unsigned ad = (rA2 ^ ((e & 2) * 16)) + (e & 1) * 1024; a[e] = trfrag(ad, ad + 512); }
    f32x4 c[4][2];
    unsigned rb[2] = {opq(rA2), opq(rA2 ^ 32)}, seen = 0;
    unsigned pk[16];
    float e0, e1, u0, u1;
    auto opf = [](float x) { asm volatile("" : "+v"(x)); return x; };
    auto micro = [&](auto nn, auto vv) {
      constexpr int n = decltype(nn)::v, d = n / 8, k = n % 8, gi = (d / 4) * 8 + d % 4, v = decltype(vv)::v;
      if constexpr (n >= 0 && n < 64) {
        if (k == 0) e0 = opf(-LOG2E * bf_lo(pk[gi]));
        if (k == 1) { e0 = opf(__builtin_amdgcn_exp2f(e0)); e1 = opf(bf_hi(pk[gi])); }
        if (k == 2) { e0 = opf(1.0f + e0); u1 = opf(-LOG2E * e1); }
        if (k == 3) { u1 = opf(__builtin_amdgcn_exp2f(u1)); u0 = opf(bf_hi(pk[gi + 4])); }
        if (k == 4) { e0 = opf(__builtin_amdgcn_rcpf(e0)); u1 = opf(1.0f + u1); }
        if (k == 5) { u1 = opf(__builtin_amdgcn_rcpf(u1)); e1 = opf(e1 * u0); }
        if (k == 6) u0 = opf(bf_lo(pk[gi]) * bf_lo(pk[gi + 4]));
        if (k == 7) hh[2 * v + d / 4][d % 4] = opq(cvtpk(u0 * e0, e1 * u1));
      }
    };
    auto dunit = [&](auto vv) {
      constexpr int v = decltype(vv)::v;
      constexpr bool last = v == 3;
      u32x4 kl = {};
      const unsigned sv = sA2 + 128 * v;
      sfor<0, 72>([&](auto nn) {
        constexpr int n = decltype(nn)::v, i = n / 12, r = n % 12;
        if (r == 0) {
          if (i == 1 && (v == 0 || v == 2) && grp) WAITV(0); else WAITV(9);
          BAR();
          if (!last) {
            if (i == 1 && v == 0 && w == 0 && __builtin_amdgcn_mbcnt_hi(~0u, __builtin_amdgcn_mbcnt_lo(~0u, 0u)) == 0) __hip_atomic_fetch_add(cnt1, 1u, __ATOMIC_RELAXED, __HIP_MEMORY_SCOPE_AGENT);
            if (i == 1 && v == 2 && w == 0 && __builtin_amdgcn_mbcnt_hi(~0u, __builtin_amdgcn_mbcnt_lo(~0u, 0u)) == 0) __hip_atomic_fetch_add(cnt2, 1u, __ATOMIC_RELAXED, __HIP_MEMORY_SCOPE_AGENT);
            if (v == 1 && i == 0 && grp) seen = __hip_atomic_load(cnt1, __ATOMIC_RELAXED, __HIP_MEMORY_SCOPE_AGENT);
            if (v == 1 && i == 1 && grp) spin16(cnt1, seen);
            if (v == 1 && (i == 3 || i == 5) && grp) {
              f32x4 sm = __builtin_bit_cast(f32x4, kl);
#pragma unroll
              for (int e = 0; e < 4; ++e) {
                sm[e] += __int_as_float(__builtin_amdgcn_update_dpp(0, __float_as_int(sm[e]), 0xB1, 0xf, 0xf, true));
                sm[e] += __int_as_float(__builtin_amdgcn_update_dpp(0, __float_as_int(sm[e]), 0x4E, 0xf, 0xf, true));
                sm[e] += __int_as_float(__builtin_amdgcn_update_dpp(0, __float_as_int(sm[e]), 0x141, 0xf, 0xf, true));
                sm[e] += __int_as_float(__builtin_amdgcn_update_dpp(0, __float_as_int(sm[e]), 0x140, 0xf, 0xf, true));
              }
              if (timeout) sm[0] = __uint_as_float(0x7fc00000u);
              const int ln = opq(__builtin_amdgcn_mbcnt_hi(~0u, __builtin_amdgcn_mbcnt_lo(~0u, 0u)));
              const int sl = (i == 5 ? 16 : 0) + 4 * (w - 4) + (ln >> 4);
              if ((ln & 15) == 0 && sl < 30)
                __builtin_amdgcn_raw_buffer_store_b64(u32x2{cvtpk(sm[0], sm[1]), cvtpk(sm[2], sm[3])}, kvf, (30 * t + sl) * 8, 0, 16);
            }
            if (v == 1 && (i == 1 || i == 3) && grp) {
              const int ln = opq(__builtin_amdgcn_mbcnt_hi(~0u, __builtin_amdgcn_mbcnt_lo(~0u, 0u)));
              const int I = 30 * t + min((i == 3 ? 16 : 0) + 4 * (w - 4) + (ln >> 4), 29), pq_ = I >= 240;
              kl = __builtin_amdgcn_raw_buffer_load_b128(kvp, ((2 * j + pq_) * 16 + (ln & 15)) * 3840 + (I - 240 * pq_) * 16, 0, 16);
            }
          }
          SCHED();
        }
        auto pieces = [&]() {
          sfor<0, 3>([&](auto mm) {
            constexpr int m = decltype(mm)::v;
            if (i == 0) dma16i<5 * SLOT + m * 8192>(wl, vA2, dA, sv + (480 + 32 * m) * 9216);
            else if (!last) dma16i<(i - 1) * SLOT + m * 8192>(wl, vA2, dA, sv + 128 + (96 * (i - 1) + 32 * m) * 9216);
            else if (i < 4) pieceA1(i - 1, m);
            else dma16i<(i - 1) * SLOT + m * 8192>(wl, vB, dB, sB[(i == 5) * 3 + m]);
          });
        };
        if (r == RY && grp) pieces();
#pragma unroll
        for (int tb = 0; tb < 2; ++tb) c[n % 4][tb] = mfma16(a[n % NA], X[2 * (n / 4) + tb], n < 4 ? f32x4{} : c[n % 4][tb]);
        constexpr int q = n + NA;
        if (q % 12 == 0) { rb[0] = opq(rb[0] + (q == 72 ? -5 * SLOT : SLOT)); rb[1] = opq(rb[1] + (q == 72 ? -5 * SLOT : SLOT)); }
        const unsigned ad = rb[(q >> 1) & 1] + ((q % 12) >> 2) * 8192 + (q & 1) * 1024;
        if (!(last && q >= 72)) a[n % NA] = trfrag(ad, ad + 512);
        if constexpr (v > 0) micro(IC<n - 2>{}, IC<v - 1>{});
        if (r == RO && !grp) pieces();
        SCHED();
      });
#pragma unroll
      for (int e = 0; e < 16; ++e) {
        const f32x4& cc = c[2 * ((e >> 1) & 1) + ((e >> 2) & 1)][e >> 3];
        pk[e] = opq(cvtpk(cc[2 * (e & 1)], cc[2 * (e & 1) + 1]));
      }
      SCHED();
    };
    dunit(IC<0>{}); dunit(IC<1>{}); dunit(IC<2>{});
    l = opq(__builtin_amdgcn_mbcnt_hi(~0u, __builtin_amdgcn_mbcnt_lo(~0u, 0u)));
    vA1 = (l >> 2) * 9216 + ((((l >> 1) & 1) ^ (tg4 & 1)) * 32) + (l & 1) * 16;
    vB = (8 * p + 4 * (l >> 5) + ((l >> 2) & 3)) * 1152 + 64 * ((l >> 4) & 1) + 16 * ((l & 3) ^ (2 * (l >> 5)));
    dunit(IC<3>{});
    l = opq(__builtin_amdgcn_mbcnt_hi(~0u, __builtin_amdgcn_mbcnt_lo(~0u, 0u)));
    h = l >> 5; g16 = (l >> 4) & 1; q4 = (l >> 2) & 3; pp = l & 3; l16 = l * 16;
    const unsigned rA1 = L0 + g16 * 2048 + h * 512 + q4 * 64 + (p ^ g16) * 32 + pp * 8;
    u32x2 kc[5];
    {
      const unsigned r0 = opq(rA1), r2 = opq(rA1 + 2 * SLOT);
      unsigned seen = __hip_atomic_load(cnt2, __ATOMIC_RELAXED, __HIP_MEMORY_SCOPE_AGENT);
      sfor<0, 36>([&](auto nn) {
        constexpr int s = decltype(nn)::v;
        if (s == 0) {
#pragma unroll
          for (int e = 0; e < NA; ++e) { const unsigned ad = r0 + (e >> 1) * 4096 + (e & 1) * 1024; a[e] = trfrag(ad, ad + 256); }
          WAITV(9); BAR();
        }
        if (s == 12) {
          WAITV(9); BAR();
          spin16(cnt2, seen);
          const unsigned ka = (p * 240 + h * 24 + min(l & 31, 23)) * 8;
#pragma unroll
          for (int e = 0; e < 5; ++e) kc[e] = __builtin_amdgcn_raw_buffer_load_b64(kvf, ka + e * 384, 0, 16);
          SCHED();
        }
        if ((s == RY && grp) || (s == RO && !grp))
          sfor<0, 3>([&](auto mm) { constexpr int m = decltype(mm)::v; dma16i<5 * SLOT + m * 8192>(wl, vB, dB, sB[6 + m]); });
#pragma unroll
        for (int tb = 0; tb < 2; ++tb) c[s % 2][tb] = mfma16(a[s % NA], X[2 * (s / 2) + tb], s < 2 ? f32x4{} : c[s % 2][tb]);
        constexpr int q = s + NA;
        if (q < 36) { const unsigned ad = (q < 24 ? r0 + (q >> 1) * 4096 : r2 + ((q - 24) >> 1) * 4096) + (q & 1) * 1024; a[s % NA] = trfrag(ad, ad + 256); }
        micro(IC<2 * s - 4>{}, IC<3>{}); micro(IC<2 * s - 3>{}, IC<3>{});
        SCHED();
      });
#pragma unroll
      for (int e = 0; e < 4; ++e)
        hh[8][e] = cvtpk(silu_f(c[0][e >> 1][2 * (e & 1)]) * c[1][e >> 1][2 * (e & 1)], silu_f(c[0][e >> 1][2 * (e & 1) + 1]) * c[1][e >> 1][2 * (e & 1) + 1]);
    }
    ST(4);
    BAR();
    ST(5);
#pragma unroll
    for (int e = 0; e < 8; ++e) lds_wr128(L0 + (tg4 * 18 + 2 * (jo + 2 * (e >> 1) + p) + (e & 1)) * 1024 + l16, hh[e]);
#pragma unroll
    for (int tb = 0; tb < 2; ++tb) lds_wr64(L0 + (tg4 * 18 + 16 * (1 - jo) + tb) * 1024 + l16 + 8 * p, u32x2{hh[8][2 * tb], hh[8][2 * tb + 1]});
    WAITL0(); BAR();
#pragma unroll
    for (int s = 0; s < 18; ++s) H[s] = lds_rd128(L0 + tg4 * 18432 + l16 + s * 1024);
    k0 = u32x4{kc[0][0], kc[0][1], kc[1][0], kc[1][1]}; k1 = u32x4{kc[2][0], kc[2][1], kc[3][0], kc[3][1]};
    k2 = u32x4{h ? 0u : kc[4][0], h ? 0u : kc[4][1], 0u, 0u};
    WAITL0();
    WAITV(0); BAR();
    sfor<0, 9>([&](auto mm) { constexpr int m = decltype(mm)::v; dma16i<(m / 3) * SLOT + (m % 3) * 8192>(wl, vB, dB, sB[m] + 256); });
  }
  ST(7);
  {
    unsigned cur = L0 + HALF, oth = L0;
    const unsigned rB = (l >> 4) * 512 + p * 256 + q4 * 64 + g16 * 32 + pp * 8;
    const auto yr = __builtin_amdgcn_make_buffer_rsrc(ws + WS_YP, 0, (unsigned)YP_BYTES, 0x00020000);
    const unsigned ybase = ((tg * 36 + 2 * p) * 16 + j) * 1024;
    u32x4 a[NB];
#pragma unroll
    for (int e = 0; e < NB; ++e) { const unsigned ad = cur + (rB ^ ((e & 1) * 32)) + (e >> 1) * 4096; a[e] = trfrag(ad, ad + 2048); }
    auto ocomp = [&]() {
    float pv[10];
#pragma unroll
    for (int i = 0; i < 5; ++i) {
      const unsigned u = *(LAS unsigned*)(uintptr_t)(L0 + PQ_OFF + i * 2048 + w * 256 + l * 4);
      pv[2 * i] = bf_lo(u); pv[2 * i + 1] = bf_hi(u);
    }
    u32x4 pos, neg; u32x2 ext;
    hedgehog(pv, h, pos, neg, ext);
    f32x16 on = {};
    on = mfma32(k0, pos, on); on = mfma32(k1, neg, on); on = mfma32(k2, u32x4{ext[0], ext[1], 0u, 0u}, on);
    const float inv = timeout ? __uint_as_float(0x7fc00000u) : __builtin_amdgcn_rcpf(xhalf_sum(h ? on[8] : 0.f) + 1e-6f);
    char* op = ws + WS_O + (size_t)(32 * tg + (l & 31)) * 1152 + (36 * j + 16 * p + 4 * h) * 2;
    const u32x2 o0 = {cvtpk(on[0] * inv, on[1] * inv), cvtpk(on[2] * inv, on[3] * inv)};
    const u32x2 o1 = {cvtpk(on[4] * inv, on[5] * inv), cvtpk(on[6] * inv, on[7] * inv)};
    const u32x2 o2 = {cvtpk(on[8] * inv, on[9] * inv), cvtpk(on[10] * inv, on[11] * inv)};
    if (p && !h) *(unsigned*)(op + 4) = o0[1]; else *(u32x2*)op = o0;
    *(u32x2*)(op + 16) = o1;
    if (!h) { if (p) *(u32x2*)(op + 32) = o2; else *(unsigned*)(op + 32) = o2[0]; }
      };
    auto period = [&](auto last_, int P) {
      constexpr bool last = decltype(last_)::v;
      unsigned rc[2], rc2[2], rn[2];
#pragma unroll
      for (int e = 0; e < 2; ++e) { const unsigned o = rB ^ (e * 32); rc[e] = opq(cur + o); rc2[e] = opq(cur + o + 2 * SLOT); rn[e] = opq(oth + o); }
      const unsigned wc = cur + w * 1024, wo_ = oth + w * 1024, sp = 256 * P;
      f32x4 acc[2][2];
      sfor<0, (last ? 18 : 36)>([&](auto nn) {
        constexpr int n = decltype(nn)::v, i = n / 12, r = n % 12;
        if (r == 0) {
          if (last) { if (i == 0) { WAITV(6); BAR(); } }
          else if (P == 3) { if (i == 0) WAITV(13); else if (i == 1) WAITV(10); else WAITV(7); BAR(); }
          else if (i > 0 || P > 0) { WAITV(11); BAR(); }
          if (!last && i == 0 && P == (grp ? 1 : 2)) ocomp();
          SCHED();
        }
        if (!last && ((r == RY && grp) || (r == RO && !grp)) && P < 3 && (i > 0 || P > 0))
          sfor<0, 3>([&](auto mm) {
            constexpr int m = decltype(mm)::v;
            if (i == 0) dma16i<2 * SLOT + m * 8192>(wo_, vB, dB, sB[6 + m] + 256 + sp);
            else dma16i<(i ? i - 1 : 0) * SLOT + m * 8192>(wc, vB, dB, sB[(i ? i - 1 : 0) * 3 + m] + 512 + sp);
          });
#pragma unroll
        for (int tb = 0; tb < 2; ++tb) acc[n & 1][tb] = mfma16(a[n % NB], H[(n % 18 & ~1) + tb], n % 18 < 2 ? f32x4{} : acc[n & 1][tb]);
        constexpr int q = n + NB;
        const unsigned ad = q < 24 ? rc[q & 1] + (q >> 1) * 4096 : q < 36 ? rc2[q & 1] + ((q - 24) >> 1) * 4096 : rn[q & 1] + ((q - 36) >> 1) * 4096;
        if (!(last && q >= 18)) a[n % NB] = trfrag(ad, ad + 2048);
        SCHED();
        if (n % 18 == 17) {
          const unsigned so = ybase + (2 * P + n / 18) * 65536;
#pragma unroll
          for (int e = 0; e < 2; ++e) {
            const u32x4 y = {cvtpk(acc[e][0][0], acc[e][0][1]), cvtpk(acc[e][0][2], acc[e][0][3]),
                             cvtpk(acc[e][1][0], acc[e][1][1]), cvtpk(acc[e][1][2], acc[e][1][3])};
            __builtin_amdgcn_raw_buffer_store_b128(y, yr, l16, so + e * 16384, YAUX);
          }
          SCHED();
        }
      });
      const unsigned tmp = cur; cur = oth; oth = tmp;
    };
#pragma unroll 1
    for (int P = 0; P < 4; ++P) period(IC<0>{}, P);
    period(IC<1>{}, 4);
  }
  ST(8);
  ST(9); ST(13); STR(14);
}

__global__ __launch_bounds__(576, 3) void k_final(const unsigned short* __restrict__ x, const char* __restrict__ ws,
                                                  unsigned short* __restrict__ out) {
  __shared__ __attribute__((aligned(1024))) char lds[36864];
  const unsigned L0 = (unsigned)(uintptr_t)(LAS char*)lds;
  const int b = blockIdx.x, tg = (b & 7) * 8 + (b >> 5), dq = (b >> 3) & 3;
  const int w = __builtin_amdgcn_readfirstlane(threadIdx.x >> 6), l = threadIdx.x & 63, g = l >> 4, hb = 9 * dq + w;
  const i32x4 dO = mkdesc(ws + WS_O, S_ * D_ * 2);
  u32x4 yp[16], wf[18];
  const auto yr = __builtin_amdgcn_make_buffer_rsrc((void*)(ws + WS_YP), 0, (unsigned)YP_BYTES, 0x00020000);
  const auto wr = __builtin_amdgcn_make_buffer_rsrc((void*)(ws + WS_WOT), 0, 36 * 18 * 1024, 0x00020000);
#pragma unroll
  for (int jj = 0; jj < YH; ++jj) yp[jj] = __builtin_amdgcn_raw_buffer_load_b128(yr, l * 16, ((tg * 36 + hb) * 16 + jj) * 1024, YLD);
  SCHED();
#pragma unroll
  for (int m = 0; m < 4; ++m) {
    const int fi = 4 * w + m, tb = fi >= 18, s = fi - 18 * tb;
    dma16(L0 + fi * 1024, (l & 15) * 1152 + g * 16, dO, (32 * tg + 16 * tb) * 1152 + 64 * s);
  }
  SCHED();
#pragma unroll
  for (int s = 0; s < 18; ++s) wf[s] = __builtin_amdgcn_raw_buffer_load_b128(wr, l * 16, (hb * 18 + s) * 1024, 0);
  SCHED();
  const size_t xo = (size_t)(32 * tg + (l & 15)) * D_ + 16 * hb + 4 * g;
  const u32x2 x0 = *(const u32x2*)(x + xo), x1 = *(const u32x2*)(x + xo + 16 * D_);
#pragma unroll
  for (int jj = YH; jj < 16; ++jj) yp[jj] = __builtin_amdgcn_raw_buffer_load_b128(yr, l * 16, ((tg * 36 + hb) * 16 + jj) * 1024, YLD);
  SCHED();
  asm volatile("s_waitcnt vmcnt(%0)" :: "i"(36 - YH) : "memory");
  BAR();
  f32x4 s0 = {}, s1 = {};
#pragma unroll
  for (int s = 0; s < 18; ++s) {
    s0 = mfma16(wf[s], lds_rd128(L0 + s * 1024 + l * 16), s0);
    s1 = mfma16(wf[s], lds_rd128(L0 + (18 + s) * 1024 + l * 16), s1);
  }
  f32x4 y0 = {}, y1 = {};
#pragma unroll
  for (int jj = 0; jj < 16; ++jj) {
    y0 += f32x4{bf_lo(yp[jj][0]), bf_hi(yp[jj][0]), bf_lo(yp[jj][1]), bf_hi(yp[jj][1])};
    y1 += f32x4{bf_lo(yp[jj][2]), bf_hi(yp[jj][2]), bf_lo(yp[jj][3]), bf_hi(yp[jj][3])};
  }
  const u32x2 r0 = {cvtpk(bf_lo(x0[0]) + s0[0] * silu_f(y0[0]), bf_hi(x0[0]) + s0[1] * silu_f(y0[1])),
                    cvtpk(bf_lo(x0[1]) + s0[2] * silu_f(y0[2]), bf_hi(x0[1]) + s0[3] * silu_f(y0[3]))};
  const u32x2 r1 = {cvtpk(bf_lo(x1[0]) + s1[0] * silu_f(y1[0]), bf_hi(x1[0]) + s1[1] * silu_f(y1[1])),
                    cvtpk(bf_lo(x1[1]) + s1[2] * silu_f(y1[2]), bf_hi(x1[1]) + s1[3] * silu_f(y1[3]))};
  *(u32x2*)(out + xo) = r0;
  *(u32x2*)(out + xo + 16 * D_) = r1;
}

extern "C" void kernel_launch(void* const* d_in, const int* in_sizes, int n_in, void* d_out, int out_size, void* d_ws,
                              size_t ws_size, hipStream_t stream) {
  const unsigned short* x = (const unsigned short*)d_in[0];
  const unsigned short* wqkv = (const unsigned short*)d_in[1];
  const unsigned short* wphi = (const unsigned short*)d_in[2];
  const unsigned short* wo = (const unsigned short*)d_in[3];
  const unsigned short* wg = (const unsigned short*)d_in[5];
  const unsigned short* wu = (const unsigned short*)d_in[6];
  const unsigned short* wd = (const unsigned short*)d_in[7];
  char* ws = (char*)d_ws;
  k_prep<<<256, 512, 0, stream>>>(x, wqkv, wo, wphi, ws);
  k_main<<<256, 512, 0, stream>>>(wg, wu, wd, ws);
  k_final<<<256, 576, 0, stream>>>(x, ws, (unsigned short*)d_out);
}
```

```cpp
#include <hip/hip_runtime.h>
#include <hip/hip_bf16.h>
#include <stdint.h>

typedef __attribute__((ext_vector_type(8))) short s16x8;
typedef __attribute__((ext_vector_type(4))) short s16x4;
typedef __attribute__((ext_vector_type(8))) __bf16 bf16x8;
typedef __attribute__((ext_vector_type(2))) __bf16 bf16x2;
typedef __attribute__((ext_vector_type(16))) float f32x16;
typedef __attribute__((ext_vector_type(4))) float f32x4;
typedef __attribute__((ext_vector_type(2))) float f32x2;
typedef __attribute__((ext_vector_type(4))) unsigned u32x4;
typedef __attribute__((ext_vector_type(2))) unsigned u32x2;
typedef __attribute__((ext_vector_type(4))) int i32x4;
#define LAS __attribute__((address_space(3)))
#define GAS __attribute__((address_space(1)))
#define DEV __device__ __forceinline__

template <int I> struct IC { static constexpr int v = I; };
template <int B, int E, class Fn> DEV void sfor(Fn&& fn) {
  if constexpr (B < E) { fn(IC<B>{}); sfor<B + 1, E>(fn); }
}

constexpr int S_ = 2048, D_ = 576, F_ = 4608;
constexpr size_t WS_XN = 0;
constexpr size_t WS_WOT = WS_XN + 64 * 36 * 1024;
constexpr size_t WS_KVP = WS_WOT + 36 * 18 * 1024;
constexpr size_t WS_O = WS_KVP + 32 * 16 * 3840;
constexpr size_t WS_CNT = WS_O + 2048 * 576 * 2;
constexpr size_t WS_KVF = WS_CNT + 32 * 256;
constexpr size_t WS_QI = WS_KVF + 16 * 4096;
constexpr size_t WS_YP = WS_QI + 16 * 6 * 24576;
constexpr size_t YP_BYTES = (size_t)16 * 64 * 36 * 1024;

DEV unsigned cvtpk(float lo, float hi) {
  f32x2 v = {lo, hi};
  return __builtin_bit_cast(unsigned, __builtin_convertvector(v, bf16x2));
}
DEV float bf_lo(unsigned u) { return __uint_as_float(u << 16); }
DEV float bf_hi(unsigned u) { return __uint_as_float(u & 0xffff0000u); }
DEV f32x16 mfma32(u32x4 a, u32x4 b, f32x16 c) {
  return __builtin_amdgcn_mfma_f32_32x32x16_bf16(__builtin_bit_cast(bf16x8, a), __builtin_bit_cast(bf16x8, b), c, 0, 0, 0);
}
DEV f32x4 mfma16(u32x4 a, u32x4 b, f32x4 c) {
  return __builtin_amdgcn_mfma_f32_16x16x32_bf16(__builtin_bit_cast(bf16x8, a), __builtin_bit_cast(bf16x8, b), c, 0, 0, 0);
}
DEV u32x2 trd(unsigned addr) {
  return __builtin_bit_cast(u32x2, __builtin_amdgcn_ds_read_tr16_b64_v4i16((LAS s16x4*)(uintptr_t)addr));
}
DEV u32x4 trfrag(unsigned a0, unsigned a1) {
  u32x2 lo = trd(a0), hi = trd(a1);
  u32x4 r = {lo[0], lo[1], hi[0], hi[1]};
  return r;
}
DEV u32x4 lds_rd128(unsigned a) { return *(LAS u32x4*)(uintptr_t)a; }
DEV u32x2 lds_rd64(unsigned a) { return *(LAS u32x2*)(uintptr_t)a; }
DEV void lds_wr128(unsigned a, u32x4 v) { *(LAS u32x4*)(uintptr_t)a = v; }
DEV void lds_wr64(unsigned a, u32x2 v) { *(LAS u32x2*)(uintptr_t)a = v; }
DEV i32x4 mkdesc(const void* p, unsigned bytes) {
  uint64_t a = (uint64_t)p;
  i32x4 d = {(int)__builtin_amdgcn_readfirstlane((unsigned)a), (int)__builtin_amdgcn_readfirstlane((unsigned)(a >> 32) & 0xffffu),
             (int)bytes, 0x00020000};
  return d;
}
DEV void dma16(unsigned lds, unsigned voff, i32x4 desc, unsigned soff) {
  asm volatile("s_mov_b32 m0, %0\n\ts_nop 0\n\tbuffer_load_dwordx4 %1, %2, %3 offen lds"
               :: "s"(lds), "v"(voff), "s"(desc), "s"(soff) : "memory");
}
template <unsigned OFF> DEV void dma16i(unsigned lds, unsigned voff, i32x4 desc, unsigned soff) {
  asm volatile("s_add_u32 m0, %0, %4\n\ts_nop 0\n\tbuffer_load_dwordx4 %1, %2, %3 offen lds"
               :: "s"(lds), "v"(voff), "s"(desc), "s"(soff), "i"(OFF) : "memory", "scc");
}
#define WAITV(n) asm volatile("s_waitcnt vmcnt(" #n ")" ::: "memory")
#define ST(k)
#define STR(k)
#define SCHED() __builtin_amdgcn_sched_barrier(0)
DEV unsigned opq(unsigned v) { asm volatile("" : "+v"(v)); return v; }
#define WAITL0() asm volatile("s_waitcnt lgkmcnt(0)" ::: "memory")
#define BAR() do { asm volatile("" ::: "memory"); __builtin_amdgcn_s_barrier(); asm volatile("" ::: "memory"); } while (0)
DEV float xhalf_max(float x) {
  auto t = __builtin_amdgcn_permlane32_swap(__float_as_uint(x), __float_as_uint(x), false, false);
  return fmaxf(__uint_as_float(t[0]), __uint_as_float(t[1]));
}
DEV float xhalf_sum(float x) {
  auto t = __builtin_amdgcn_permlane32_swap(__float_as_uint(x), __float_as_uint(x), false, false);
  return __uint_as_float(t[0]) + __uint_as_float(t[1]);
}
constexpr float LOG2E = 1.4426950408889634f;
DEV float silu_f(float g) { return g * __builtin_amdgcn_rcpf(1.0f + __builtin_amdgcn_exp2f(-LOG2E * g)); }

DEV void hedgehog(const float (&pv)[10], int h, u32x4& pos, u32x4& neg, u32x2& ext) {
  float mx = pv[0], mn = pv[0];
#pragma unroll
  for (int r = 1; r < 8; ++r) { mx = fmaxf(mx, pv[r]); mn = fminf(mn, pv[r]); }
  if (h == 0) { mx = fmaxf(mx, fmaxf(pv[8], pv[9])); mn = fminf(mn, fminf(pv[8], pv[9])); }
  mx = xhalf_max(mx); mn = -xhalf_max(-mn);
  float ep[10], en[10], sp = 0.f, sn = 0.f;
#pragma unroll
  for (int r = 0; r < 10; ++r) {
    ep[r] = __builtin_amdgcn_exp2f((pv[r] - mx) * LOG2E);
    en[r] = __builtin_amdgcn_exp2f((mn - pv[r]) * LOG2E);
    if (r >= 8) { ep[r] = h ? 0.f : ep[r]; en[r] = h ? 0.f : en[r]; }
    sp += ep[r]; sn += en[r];
  }
  float ip = __builtin_amdgcn_rcpf(xhalf_sum(sp)), in = __builtin_amdgcn_rcpf(xhalf_sum(sn));
#pragma unroll
  for (int i = 0; i < 4; ++i) {
    pos[i] = cvtpk(ep[2 * i] * ip, ep[2 * i + 1] * ip);
    neg[i] = cvtpk(en[2 * i] * in, en[2 * i + 1] * in);
  }
  ext[0] = cvtpk(ep[8] * ip, ep[9] * ip);
  ext[1] = cvtpk(en[8] * in, en[9] * in);
}

__global__ __launch_bounds__(512) void k_prep(const unsigned short* __restrict__ x, const unsigned short* __restrict__ wqkv,
                                              const unsigned short* __restrict__ wo,
                                              const unsigned short* __restrict__ wphi, char* __restrict__ ws) {
  const int b = blockIdx.x, w = threadIdx.x >> 6, l = threadIdx.x & 63;
  if (w < 2) {
    const int tok = 8 * b + 4 * w + (l >> 4), i = l & 15;
    u32x2 v[9];
#pragma unroll
    for (int u = 0; u < 9; ++u) v[u] = *(const u32x2*)(x + (size_t)tok * D_ + 4 * (i + 16 * u));
    float f[36], sum = 0.f;
#pragma unroll
    for (int u = 0; u < 9; ++u) {
      f[4 * u] = bf_lo(v[u][0]); f[4 * u + 1] = bf_hi(v[u][0]); f[4 * u + 2] = bf_lo(v[u][1]); f[4 * u + 3] = bf_hi(v[u][1]);
      sum += (f[4 * u] + f[4 * u + 1]) + (f[4 * u + 2] + f[4 * u + 3]);
    }
#pragma unroll
    for (int m = 1; m < 16; m <<= 1) sum += __shfl_xor(sum, m);
    const float mean = sum * (1.0f / D_);
    float sq = 0.f;
#pragma unroll
    for (int e = 0; e < 36; ++e) { f[e] -= mean; sq += f[e] * f[e]; }
#pragma unroll
    for (int m = 1; m < 16; m <<= 1) sq += __shfl_xor(sq, m);
    const float rs = 1.0f / sqrtf(sq * (1.0f / D_) + 1e-5f);
    char* dst = ws + WS_XN + (size_t)(tok >> 5) * 36864 + (tok & 31) * 16;
#pragma unroll
    for (int u = 0; u < 9; ++u) {
      const int c8 = i + 16 * u, c = c8 >> 1;
      u32x2 o = {cvtpk(f[4 * u] * rs, f[4 * u + 1] * rs), cvtpk(f[4 * u + 2] * rs, f[4 * u + 3] * rs)};
      *(u32x2*)(dst + (c >> 1) * 1024 + (c & 1) * 512 + (c8 & 1) * 8) = o;
    }
  } else if (w >= 5) {
    u32x4 v[3];
#pragma unroll
    for (int m = 0; m < 3; ++m) {
      const int id = 576 * b + (int)threadIdx.x - 320 + 192 * m, jj = id / 9216, g = (id % 9216) / 1536;
      const int c = min(id % 1536, 1439), k16 = c / 240, r = c % 240, seg = r / 80, pos = (r % 80) / 5, ch = r % 5;
      const int row = 96 * g + 16 * k16 + 8 * ((pos >> 1) & 1) + 4 * (pos & 1) + (pos >> 2);
      const int off = min(row * 3456 + seg * 1152 + ((72 * jj) & ~15) + 16 * ch, D_ * 3 * D_ * 2 - 16);
      v[m] = *(const u32x4*)((const char*)wqkv + off);
    }
#pragma unroll
    for (int m = 0; m < 3; ++m) {
      const int id = 576 * b + (int)threadIdx.x - 320 + 192 * m, g = (id % 9216) / 1536;
      if (!(id % 1536 >= 1440 && (g == 2 || g == 3))) *(u32x4*)(ws + WS_QI + (size_t)id * 16) = v[m];
    }
  } else {
    for (int fi = 3 * b + (w - 2); fi < 648 + 64; fi += 768) {
      unsigned short e[8];
      char* dst;
      if (fi < 648) {
        const int hb = fi / 18, s = fi % 18, i = l & 15, g = l >> 4, gi = i >> 2;
        const int d = 16 * hb + 8 * (gi & 1) + 4 * (gi >> 1) + (i & 3);
#pragma unroll
        for (int jj = 0; jj < 8; ++jj) e[jj] = wo[(size_t)(32 * s + 8 * g + jj) * D_ + d];
        dst = ws + WS_WOT + (size_t)fi * 1024;
      } else {
        const int q = fi - 648, head = q >> 1, s = q & 1, f = l & 31, h = l >> 5;
#pragma unroll
        for (int jj = 0; jj < 8; ++jj) {
          const int d = 16 * s + 8 * (jj >> 2) + 4 * h + (jj & 3) - 2 * (head & 1);
          const bool ok = f < 18 && d >= 0 && d < 18;
          const unsigned short t = wphi[head * 324 + (ok ? d * 18 + f : 0)];
          e[jj] = ok ? t : (unsigned short)0;
        }
        dst = ws + WS_QI + (size_t)((head >> 1) * 6 + 2 + (head & 1)) * 24576 + 23040;
        if (s) {
          *(u32x2*)(dst + 1024 + l * 8) = u32x2{e[0] | ((unsigned)e[1] << 16), e[2] | ((unsigned)e[3] << 16)};
          continue;
        }
      }
      u32x4 o = {e[0] | ((unsigned)e[1] << 16), e[2] | ((unsigned)e[3] << 16), e[4] | ((unsigned)e[5] << 16), e[6] | ((unsigned)e[7] << 16)};
      *(u32x4*)(dst + l * 16) = o;
    }
  }
  if (b == 0 && threadIdx.x < 32) *(unsigned*)(ws + WS_CNT + threadIdx.x * 256) = 0u;
}

constexpr unsigned SLOT = 24576, HALF = 3 * SLOT, SPARE = 6 * SLOT, LDS_MAIN = 163840;
#define JMAP (4 * (b & 3) + ((b >> 3) & 3))
#define TMAP (((b >> 2) & 1) * 8 + (b >> 5))
#define RY 1
#define RO 8
#define PFC 1
#define PFW 1
#define NA 3
#define NB 6
#define NQ 6
#define YH 0
#define YLD 2
#define YAUX 16

__global__ __launch_bounds__(512, 2) void k_main(const unsigned short* __restrict__ wg, const unsigned short* __restrict__ wu,
                                                 const unsigned short* __restrict__ wd, char* __restrict__ ws) {
  __shared__ __attribute__((aligned(1024))) char lds[LDS_MAIN];
  const unsigned L0 = (unsigned)(uintptr_t)(LAS char*)lds;
  const int b = blockIdx.x, tid = threadIdx.x;
  const int j = JMAP, t = TMAP, jo = j & 1;
  const int w = __builtin_amdgcn_readfirstlane(tid >> 6), tg4 = w >> 1, p = w & 1;
  int l = tid & 63, h = l >> 5, g16 = (l >> 4) & 1, q4 = (l >> 2) & 3, pp = l & 3;
  const int tg = 4 * t + tg4;
  GAS unsigned* const cnt1 = (GAS unsigned*)(ws + WS_CNT + j * 256);
  GAS unsigned* const cnt2 = cnt1 + 1024;

  const i32x4 dW = mkdesc(ws, (unsigned)WS_YP), dA = mkdesc(p ? wu : wg, D_ * F_ * 2), dB = mkdesc(wd, D_ * F_ * 2);
  unsigned l16 = l * 16;
  const unsigned vA2 = (l >> 3) * 9216 + ((((l >> 1) & 3) ^ (((l >> 4) & 1) + 2 * p)) * 32) + (l & 1) * 16;
  unsigned vA1 = 0, vB = 0;
  auto pieceX = [&](int half, int m) {
    const int n = 9 * w + m, tq = w >> 1, s = 18 * half + n - 18 * tq;
    dma16(L0 + n * 1024, l16, dW, (unsigned)WS_XN + ((4 * t + tq) * 36 + s) * 1024);
  };
  auto pieceQ = [&](int g, int m) {
    dma16(L0 + (3 + g % 3) * SLOT + (w + 8 * m) * 1024, l16, dW, (unsigned)WS_QI + (j * 6 + g) * SLOT + (w + 8 * m) * 1024);
  };
  auto pieceA2 = [&](int v, int i, int m) {
    dma16(L0 + i * SLOT + (w + 8 * m) * 1024, vA2, dA,
          (96 * i + 16 * ((w >> 2) + 2 * m) + 8 * (tg4 & 1)) * 9216 + (288 * j + 32 * jo + 64 * v) * 2);
  };
  auto pieceA1 = [&](int i, int m) {
    dma16(L0 + i * SLOT + (w + 8 * m) * 1024, vA1, dA, (192 * i + 16 * (tg4 + 4 * m)) * 9216 + (288 * j + 256 * (1 - jo)) * 2);
  };
  const bool grp = w >= 4;
  const unsigned wl = L0 + w * 1024;
  const unsigned sA2 = (16 * (w >> 2) + 8 * (tg4 & 1)) * 9216 + (288 * j + 32 * jo) * 2;
  unsigned sB[9];
#pragma unroll
  for (int e = 0; e < 9; ++e) {
    const int it = 12 * (e / 3) + tg4 + 4 * (e % 3), dp = it >= 18;
    sB[e] = (288 * j + 16 * (it - 18 * dp)) * 1152 + 128 * dp;
  }
  bool timeout = false;
  auto spin16 = [&](GAS unsigned* c, unsigned seen) {
    unsigned spins = 0;
    while (__builtin_amdgcn_readfirstlane(seen) < 16u) {
      __builtin_amdgcn_s_sleep(2);
      seen = __hip_atomic_load(c, __ATOMIC_RELAXED, __HIP_MEMORY_SCOPE_AGENT);
      if (++spins > (1u << 22)) { timeout = true; break; }
    }
  };

  STR(15); ST(0);
  u32x4 X[36], wp0, wp1;
#pragma unroll
  for (int m = 0; m < 9; ++m) pieceX(0, m);
#pragma unroll
  for (int m = 0; m < 9; ++m) pieceQ(m / 3, m % 3);
  WAITV(3); BAR();
  const unsigned rx = opq(L0 + tg4 * 18432 + l16);
#pragma unroll
  for (int s = 0; s < 18; ++s) X[s] = lds_rd128(rx + s * 1024);
  WAITL0(); BAR();
  if (PFC) {
    uint64_t pc;
    asm volatile("s_getpc_b64 %0" : "=s"(pc));
    asm volatile("s_mov_b32 m0, %0\n\ts_nop 0\n\tglobal_load_lds_dword %1, %2"
                 :: "s"(L0 + SPARE + w * 256), "v"(min(tid, 279) * 128), "s"(pc) : "memory");
  }
  if (PFW)
    asm volatile("s_mov_b32 m0, %0\n\ts_nop 0\n\tbuffer_load_dword %1, %2, %3 offen lds"
                 :: "s"(L0 + SPARE + 2048 + w * 256), "v"(((t & 7) * 324 + min(tid, 323)) * 128), "s"(dB), "s"(288 * j * 1152) : "memory");
  ST(1);

  f32x16 aq = {}, ak = {}, av = {};
  {
    const unsigned rq = L0 + 3 * SLOT + (4 * q4 + 2 * h) * 80 + ((72 * j) & 15) + 32 * p + 32 * g16 + 8 * pp;
#define QADDR(n) (rb[((n) / 18) & 1] + (((n) % 18) / 3) * 3840 + ((n) % 3) * 1280)
    u32x4 a[NQ];
    unsigned rb[2] = {rq, rq};
    sfor<0, 108>([&](auto nn) {
      constexpr int n = decltype(nn)::v, g = n / 18, r = n % 18;
      if (r == 0) {
        if (g == 1 || g == 4) WAITV(9);
        if (g == 3) WAITV(3);
        if (g) BAR();
        rb[(g + 1) & 1] = opq(rq + ((g + 1) % 3) * SLOT);
        if (g == 3) {
#pragma unroll
          for (int s = 18; s < 36; ++s) X[s] = lds_rd128(rx + (s - 18) * 1024);
          const unsigned wa = L0 + (p ? 3 : 5) * SLOT + 23040;
          wp0 = lds_rd128(wa + l16);
          const u32x2 w1 = lds_rd64(wa + 1024 + l * 8);
          wp1 = u32x4{w1[0], w1[1], 0u, 0u};
        }
        if (g == 0 || g == 3) {
#pragma unroll
          for (int e = 0; e < NQ; ++e) a[e] = trfrag(QADDR(n + e), QADDR(n + e) + 80);
        }
        if (g == 3) { WAITL0(); BAR(); }
        SCHED();
      }
      if (g == 0 && r < 9) pieceX(1, r);
      if ((g == 1 || g == 2) && r < 3) pieceQ(g + 2, r);
      if (g == 3 && r < 3) pieceQ(5, r);
      if (g == 3 && r >= 3 && r < 12) pieceA2(0, (r - 3) / 3, r % 3);
      if (g >= 4 && r % 6 == 1) pieceA2(0, g - 1, r / 6);
      constexpr int s = n / 3;
      if (n % 3 == 0) aq = mfma32(a[n % NQ], X[s], aq);
      else if (n % 3 == 1) ak = mfma32(a[n % NQ], X[s], ak);
      else av = mfma32(X[s], a[n % NQ], av);
      if (n + NQ < 108 && (n + NQ < 54 || n >= 54)) a[n % NQ] = trfrag(QADDR(n + NQ), QADDR(n + NQ) + 80);
      if (n == 107 || n == 53) asm volatile("" : "+v"(aq), "+v"(ak), "+v"(av));
      SCHED();
    });
  }
  ST(2);
  constexpr unsigned PQ_OFF = 5 * SLOT + 8 * 3840;
  {
    auto phi = [&](const f32x16& c) {
      u32x4 b0 = {cvtpk(c[0], c[1]), cvtpk(c[2], c[3]), cvtpk(c[4], c[5]), cvtpk(c[6], c[7])};
      u32x4 b1 = {cvtpk(c[8], c[9]), cvtpk(c[10], c[11]), 0u, 0u};
      b0[0] = (p && !h) ? 0u : b0[0];
      b1[0] = h ? 0u : b1[0];
      b1[1] = (p && !h) ? b1[1] : 0u;
      f32x16 z = {};
      z = mfma32(wp0, b0, z);
      return mfma32(wp1, b1, z);
    };
    const f32x16 pq = phi(aq), pk = phi(ak);
#pragma unroll
    for (int i = 0; i < 5; ++i) *(LAS unsigned*)(uintptr_t)(L0 + PQ_OFF + i * 2048 + tid * 4) = cvtpk(pq[2 * i], pq[2 * i + 1]);
    float pv[10];
#pragma unroll
    for (int i = 0; i < 5; ++i) { const unsigned u = cvtpk(pk[2 * i], pk[2 * i + 1]); pv[2 * i] = bf_lo(u); pv[2 * i + 1] = bf_hi(u); }
    u32x4 pos, neg; u32x2 ext;
    hedgehog(pv, h, pos, neg, ext);
    BAR();
    const unsigned R = L0 + 5 * SLOT + w * 3840, tk = l & 31;
    lds_wr64(R + tk * 64 + 8 * h, u32x2{pos[0], pos[1]});
    lds_wr64(R + tk * 64 + 16 + 8 * h, u32x2{pos[2], pos[3]});
    lds_wr64(R + tk * 64 + 32 + 8 * h, u32x2{neg[0], neg[1]});
    lds_wr64(R + tk * 64 + 48 + 8 * h, u32x2{neg[2], neg[3]});
    lds_wr64(R + 2048 + l * 8, ext);
    asm volatile("" ::: "memory");
    f32x16 kv0 = {}, kv1 = {};
#pragma unroll
    for (int s = 0; s < 2; ++s) {
      u32x4 vb;
#pragma unroll
      for (int i = 0; i < 4; ++i) vb[i] = (tk == 20) ? 0x3f803f80u : cvtpk(av[8 * s + 2 * i], av[8 * s + 2 * i + 1]);
      const unsigned r0 = 16 * s + 4 * h + q4;
      kv0 = mfma32(trfrag(R + r0 * 64 + 32 * g16 + 8 * pp, R + (r0 + 8) * 64 + 32 * g16 + 8 * pp), vb, kv0);
      kv1 = mfma32(trfrag(R + 2048 + r0 * 8, R + 2048 + (r0 + 8) * 8), vb, kv1);
    }
    asm volatile("" ::: "memory");
    if (tk < 24) {
      const unsigned Rl = R + (h * 24 + tk) * 16;
#pragma unroll
      for (int qd = 0; qd < 4; ++qd)
        lds_wr128(Rl + qd * 768, __builtin_bit_cast(u32x4, f32x4{kv0[4 * qd], kv0[4 * qd + 1], kv0[4 * qd + 2], kv0[4 * qd + 3]}));
      lds_wr128(Rl + 3072, __builtin_bit_cast(u32x4, f32x4{kv1[0], kv1[1], kv1[2], kv1[3]}));
    }
    WAITL0(); WAITV(9); BAR();
    if (tid >= 272) {
      const int rem = tid - 272;
#pragma unroll
      for (int pq_ = 0; pq_ < 2; ++pq_) {
        f32x4 sum = {};
#pragma unroll
        for (int e = 0; e < 4; ++e) sum += __builtin_bit_cast(f32x4, lds_rd128(L0 + 5 * SLOT + (2 * e + pq_) * 3840 + rem * 16));
        __builtin_amdgcn_raw_buffer_store_b128(__builtin_bit_cast(u32x4, sum),
            __builtin_amdgcn_make_buffer_rsrc(ws + WS_KVP, 0, 32 * 16 * 3840, 0x00020000),
            ((2 * j + pq_) * 16 + t) * 3840 + rem * 16, 0, 16);
      }
    }
    WAITL0();
  }

  ST(3);
  u32x4 H[18], k0, k1, k2;
  {
    u32x4 a[NA], hh[9];
    auto act = [&](const f32x16& c) {
      u32x4 r;
#pragma unroll
      for (int i = 0; i < 4; ++i) r[i] = cvtpk(silu_f(c[2 * i]) * c[8 + 2 * i], silu_f(c[2 * i + 1]) * c[9 + 2 * i]);
      return r;
    };
    const unsigned rA2 = L0 + h * 2048 + g16 * 1024 + q4 * 128 + (((2 * p) ^ ((q4 >> 1) + 2 * g16)) * 32) + pp * 8;
    const auto kvp = __builtin_amdgcn_make_buffer_rsrc(ws + WS_KVP, 0, 32 * 16 * 3840, 0x00020000);
    const auto kvf = __builtin_amdgcn_make_buffer_rsrc(ws + WS_KVF + j * 4096, 0, 4096, 0x00020000);
#pragma unroll
    for (int e = 0; e < NA; ++e) { const unsigned ad = (rA2 ^ ((e & 1) * 32)) + (e >> 1) * 4096; a[e] = trfrag(ad, ad + 512); }
    f32x16 c0 = {}, c1 = {};
    unsigned pk[16];
    float e0, e1, u0, u1;
    auto opf = [](float x) { asm volatile("" : "+v"(x)); return x; };
    auto micro = [&](auto nn, auto vv) {
      constexpr int n = decltype(nn)::v, d = n / 8, k = n % 8, gi = (d / 4) * 8 + d % 4, v = decltype(vv)::v;
      if constexpr (n >= 0 && n < 64) {
        if (k == 0) e0 = opf(-LOG2E * bf_lo(pk[gi]));
        if (k == 1) { e0 = opf(__builtin_amdgcn_exp2f(e0)); e1 = opf(bf_hi(pk[gi])); }
        if (k == 2) { e0 = opf(1.0f + e0); u1 = opf(-LOG2E * e1); }
        if (k == 3) { u1 = opf(__builtin_amdgcn_exp2f(u1)); u0 = opf(bf_hi(pk[gi + 4])); }
        if (k == 4) { e0 = opf(__builtin_amdgcn_rcpf(e0)); u1 = opf(1.0f + u1); }
        if (k == 5) { u1 = opf(__builtin_amdgcn_rcpf(u1)); e1 = opf(e1 * u0); }
        if (k == 6) u0 = opf(bf_lo(pk[gi]) * bf_lo(pk[gi + 4]));
        if (k == 7) hh[2 * v + d / 4][d % 4] = opq(cvtpk(u0 * e0, e1 * u1));
      }
    };
    auto dunit = [&](auto vv) {
      constexpr int v = decltype(vv)::v;
      constexpr bool last = v == 3;
      unsigned rb[2][2], seen = 0;
      u32x4 kl = {}, kl2 = {};
      const unsigned sv = sA2 + 128 * v;
      rb[0][0] = opq(rA2); rb[0][1] = opq(rA2 ^ 32);
      sfor<0, 72>([&](auto nn) {
        constexpr int n = decltype(nn)::v, i = n / 12, r = n % 12;
        if (r == 0) {
          if (((i == 1 && v == 0) || (i == 0 && v == 2)) && grp) WAITV(0); else WAITV(9);
          BAR();
          if (!last) {
            if (i == 1 && v == 0 && tid == 0) __hip_atomic_fetch_add(cnt1, 1u, __ATOMIC_RELAXED, __HIP_MEMORY_SCOPE_AGENT);
            if (i == 0 && v == 2 && tid == 0) __hip_atomic_fetch_add(cnt2, 1u, __ATOMIC_RELAXED, __HIP_MEMORY_SCOPE_AGENT);
            if (v == 1 && grp) {
              if (i == 0) seen = __hip_atomic_load(cnt1, __ATOMIC_RELAXED, __HIP_MEMORY_SCOPE_AGENT);
              if (i == 2) {
                spin16(cnt1, seen);
                const int I = 30 * t + min((tid - 256) >> 3, 29), pq_ = I >= 240;
                const int o = ((2 * j + pq_) * 16 + (tid & 7)) * 3840 + (I - 240 * pq_) * 16;
                kl = __builtin_amdgcn_raw_buffer_load_b128(kvp, o, 0, 16);
                kl2 = __builtin_amdgcn_raw_buffer_load_b128(kvp, o, 8 * 3840, 16);
              }
              if (i == 4) {
                f32x4 sm = __builtin_bit_cast(f32x4, kl) + __builtin_bit_cast(f32x4, kl2);
#pragma unroll
                for (int e = 0; e < 4; ++e) {
                  sm[e] += __int_as_float(__builtin_amdgcn_update_dpp(0, __float_as_int(sm[e]), 0xB1, 0xf, 0xf, true));
                  sm[e] += __int_as_float(__builtin_amdgcn_update_dpp(0, __float_as_int(sm[e]), 0x4E, 0xf, 0xf, true));
                  sm[e] += __int_as_float(__builtin_amdgcn_update_dpp(0, __float_as_int(sm[e]), 0x141, 0xf, 0xf, true));
                }
                if (timeout) sm[0] = __uint_as_float(0x7fc00000u);
                if ((tid & 7) == 0 && tid < 496)
                  __builtin_amdgcn_raw_buffer_store_b64(u32x2{cvtpk(sm[0], sm[1]), cvtpk(sm[2], sm[3])}, kvf, (30 * t + ((tid - 256) >> 3)) * 8, 0, 16);
              }
            }
          }
          if (i < 5) { rb[(i + 1) & 1][0] = opq(rA2 + (i + 1) * SLOT); rb[(i + 1) & 1][1] = opq((rA2 ^ 32) + (i + 1) * SLOT); }
          else { rb[0][0] = opq(rA2); rb[0][1] = opq(rA2 ^ 32); }
          SCHED();
        }
        auto pieces = [&]() {
          sfor<0, 3>([&](auto mm) {
            constexpr int m = decltype(mm)::v;
            if (i == 0) dma16i<5 * SLOT + m * 8192>(wl, vA2, dA, sv + (480 + 32 * m) * 9216);
            else if (!last) dma16i<(i - 1) * SLOT + m * 8192>(wl, vA2, dA, sv + 128 + (96 * (i - 1) + 32 * m) * 9216);
            else if (i < 4) pieceA1(i - 1, m);
            else dma16i<(i - 1) * SLOT + m * 8192>(wl, vB, dB, sB[(i == 5) * 3 + m]);
          });
        };
        if (r == RY && grp) pieces();
        if (n < 2) { const f32x16 z = {}; if (n == 0) c0 = mfma32(a[0], X[0], z); else c1 = mfma32(a[1], X[0], z); }
        else if (n % 2 == 0) c0 = mfma32(a[n % NA], X[n / 2], c0);
        else c1 = mfma32(a[n % NA], X[n / 2], c1);
        constexpr int q = n + NA;
        const unsigned ad = rb[(q / 12) & 1][q & 1] + ((q % 12) >> 1) * 4096;
        if (!(last && q >= 72)) a[n % NA] = trfrag(ad, ad + 512);
        if constexpr (v > 0) micro(IC<n - 2>{}, IC<v - 1>{});
        if (r == RO && !grp) pieces();
        SCHED();
      });
#pragma unroll
      for (int e = 0; e < 8; ++e) { pk[e] = opq(cvtpk(c0[2 * e], c0[2 * e + 1])); pk[8 + e] = opq(cvtpk(c1[2 * e], c1[2 * e + 1])); }
      SCHED();
    };
    dunit(IC<0>{}); dunit(IC<1>{}); dunit(IC<2>{});
    l = opq(__builtin_amdgcn_mbcnt_hi(~0u, __builtin_amdgcn_mbcnt_lo(~0u, 0u)));
    vA1 = (l >> 2) * 9216 + ((((l >> 1) & 1) ^ p) * 32) + (l & 1) * 16;
    vB = (8 * p + 4 * (l >> 5) + ((l >> 2) & 3)) * 1152 + 64 * ((l >> 4) & 1) + 16 * (l & 3);
    dunit(IC<3>{});
    l = opq(__builtin_amdgcn_mbcnt_hi(~0u, __builtin_amdgcn_mbcnt_lo(~0u, 0u)));
    h = l >> 5; g16 = (l >> 4) & 1; q4 = (l >> 2) & 3; pp = l & 3; l16 = l * 16;
    const unsigned rA1 = L0 + g16 * 1024 + h * 512 + q4 * 64 + (p ^ g16) * 32 + pp * 8;
    u32x2 kc[5];
    {
      const unsigned r0 = opq(rA1), r2 = opq(rA1 + 2 * SLOT);
      unsigned seen = __hip_atomic_load(cnt2, __ATOMIC_RELAXED, __HIP_MEMORY_SCOPE_AGENT);
      c0 = f32x16{};
      sfor<0, 36>([&](auto nn) {
        constexpr int s = decltype(nn)::v;
        if (s == 0) {
#pragma unroll
          for (int e = 0; e < NA; ++e) a[e] = trfrag(r0 + e * 2048, r0 + e * 2048 + 256);
          WAITV(9); BAR();
        }
        if (s == 12) {
          WAITV(9); BAR();
          spin16(cnt2, seen);
          const unsigned ka = (p * 240 + h * 24 + min(l & 31, 23)) * 8;
#pragma unroll
          for (int e = 0; e < 5; ++e) kc[e] = __builtin_amdgcn_raw_buffer_load_b64(kvf, ka + e * 384, 0, 16);
          SCHED();
        }
        if ((s == RY && grp) || (s == RO && !grp))
          sfor<0, 3>([&](auto mm) { constexpr int m = decltype(mm)::v; dma16i<5 * SLOT + m * 8192>(wl, vB, dB, sB[6 + m]); });
        c0 = mfma32(a[s % NA], X[s], c0);
        constexpr int q = s + NA;
        if (q < 36) { const unsigned ad = q < 24 ? r0 + q * 2048 : r2 + (q - 24) * 2048; a[s % NA] = trfrag(ad, ad + 256); }
        micro(IC<2 * s - 4>{}, IC<3>{}); micro(IC<2 * s - 3>{}, IC<3>{});
        SCHED();
      });
      hh[8] = act(c0);
    }
    ST(4);
    BAR();
    ST(5);
#pragma unroll
    for (int e = 0; e < 8; ++e) lds_wr128(L0 + (tg4 * 18 + 2 * jo + 4 * (e >> 1) + 2 * p + (e & 1)) * 1024 + l16, hh[e]);
    lds_wr128(L0 + (tg4 * 18 + 16 * (1 - jo) + p) * 1024 + l16, hh[8]);
    WAITL0(); BAR();
#pragma unroll
    for (int s = 0; s < 18; ++s) H[s] = lds_rd128(L0 + tg4 * 18432 + l16 + s * 1024);
    k0 = u32x4{kc[0][0], kc[0][1], kc[1][0], kc[1][1]}; k1 = u32x4{kc[2][0], kc[2][1], kc[3][0], kc[3][1]};
    k2 = u32x4{h ? 0u : kc[4][0], h ? 0u : kc[4][1], 0u, 0u};
    WAITL0();
    WAITV(0); BAR();
    sfor<0, 9>([&](auto mm) { constexpr int m = decltype(mm)::v; dma16i<(m / 3) * SLOT + (m % 3) * 8192>(wl, vB, dB, sB[m] + 256); });
  }
  ST(7);
  {
    unsigned cur = L0 + HALF, oth = L0;
    const unsigned rB = h * 512 + p * 256 + q4 * 64 + g16 * 32 + pp * 8;
    const auto yr = __builtin_amdgcn_make_buffer_rsrc(ws + WS_YP, 0, (unsigned)YP_BYTES, 0x00020000);
    const unsigned ybase = ((tg * 36 + 2 * p) * 16 + j) * 1024;
    u32x4 a[NB];
#pragma unroll
    for (int e = 0; e < NB; ++e) a[e] = trfrag(cur + rB + e * 2048, cur + rB + e * 2048 + 1024);
    auto ocomp = [&]() {
    float pv[10];
#pragma unroll
    for (int i = 0; i < 5; ++i) {
      const unsigned u = *(LAS unsigned*)(uintptr_t)(L0 + PQ_OFF + i * 2048 + w * 256 + l * 4);
      pv[2 * i] = bf_lo(u); pv[2 * i + 1] = bf_hi(u);
    }
    u32x4 pos, neg; u32x2 ext;
    hedgehog(pv, h, pos, neg, ext);
    f32x16 on = {};
    on = mfma32(k0, pos, on); on = mfma32(k1, neg, on); on = mfma32(k2, u32x4{ext[0], ext[1], 0u, 0u}, on);
    const float inv = timeout ? __uint_as_float(0x7fc00000u) : __builtin_amdgcn_rcpf(xhalf_sum(h ? on[8] : 0.f) + 1e-6f);
    char* op = ws + WS_O + (size_t)(32 * tg + (l & 31)) * 1152 + (36 * j + 16 * p + 4 * h) * 2;
    const u32x2 o0 = {cvtpk(on[0] * inv, on[1] * inv), cvtpk(on[2] * inv, on[3] * inv)};
    const u32x2 o1 = {cvtpk(on[4] * inv, on[5] * inv), cvtpk(on[6] * inv, on[7] * inv)};
    const u32x2 o2 = {cvtpk(on[8] * inv, on[9] * inv), cvtpk(on[10] * inv, on[11] * inv)};
    if (p && !h) *(unsigned*)(op + 4) = o0[1]; else *(u32x2*)op = o0;
    *(u32x2*)(op + 16) = o1;
    if (!h) { if (p) *(u32x2*)(op + 32) = o2; else *(unsigned*)(op + 32) = o2[0]; }
      };
    auto period = [&](auto last_, int P) {
      constexpr bool last = decltype(last_)::v;
      const unsigned rc = opq(cur + rB), rc2 = opq(cur + rB + 2 * SLOT), rn = opq(oth + rB);
      const unsigned wc = cur + w * 1024, wo_ = oth + w * 1024, sp = 256 * P;
      f32x16 acc = {};
      sfor<0, (last ? 18 : 36)>([&](auto nn) {
        constexpr int n = decltype(nn)::v, i = n / 12, r = n % 12;
        if (r == 0) {
          if (last) { if (i == 0) { WAITV(6); BAR(); } }
          else if (P == 3) { if (i == 0) WAITV(13); else if (i == 1) WAITV(10); else WAITV(7); BAR(); }
          else if (i > 0 || P > 0) { WAITV(11); BAR(); }
          if (!last && i == 0 && P == (grp ? 1 : 2)) ocomp();
          SCHED();
        }
        if (!last && ((r == RY && grp) || (r == RO && !grp)) && P < 3 && (i > 0 || P > 0))
          sfor<0, 3>([&](auto mm) {
            constexpr int m = decltype(mm)::v;
            if (i == 0) dma16i<2 * SLOT + m * 8192>(wo_, vB, dB, sB[6 + m] + 256 + sp);
            else dma16i<(i ? i - 1 : 0) * SLOT + m * 8192>(wc, vB, dB, sB[(i ? i - 1 : 0) * 3 + m] + 512 + sp);
          });
        if (n % 18 == 0) { const f32x16 z = {}; acc = mfma32(a[n % NB], H[0], z); }
        else acc = mfma32(a[n % NB], H[n % 18], acc);
        constexpr int q = n + NB;
        const unsigned ad = q < 24 ? rc + q * 2048 : q < 36 ? rc2 + (q - 24) * 2048 : rn + (q - 36) * 2048;
        if (!(last && q >= 18)) a[n % NB] = trfrag(ad, ad + 1024);
        SCHED();
        if (n % 18 == 17) {
          const unsigned so = ybase + (2 * P + n / 18) * 65536;
#pragma unroll
          for (int e = 0; e < 2; ++e) {
            const auto s0 = __builtin_amdgcn_permlane16_swap(cvtpk(acc[8 * e], acc[8 * e + 1]), cvtpk(acc[8 * e + 4], acc[8 * e + 5]), false, false);
            const auto s1 = __builtin_amdgcn_permlane16_swap(cvtpk(acc[8 * e + 2], acc[8 * e + 3]), cvtpk(acc[8 * e + 6], acc[8 * e + 7]), false, false);
            const u32x4 y = {s0[0], s1[0], s0[1], s1[1]};
            __builtin_amdgcn_raw_buffer_store_b128(y, yr, l16, so + e * 16384, YAUX);
          }
          SCHED();
        }
      });
      const unsigned tmp = cur; cur = oth; oth = tmp;
    };
#pragma unroll 1
    for (int P = 0; P < 4; ++P) period(IC<0>{}, P);
    period(IC<1>{}, 4);
  }
  ST(8);
  ST(9); ST(13); STR(14);
}

__global__ __launch_bounds__(576, 3) void k_final(const unsigned short* __restrict__ x, const char* __restrict__ ws,
                                                  unsigned short* __restrict__ out) {
  __shared__ __attribute__((aligned(1024))) char lds[36864];
  const unsigned L0 = (unsigned)(uintptr_t)(LAS char*)lds;
  const int b = blockIdx.x, tg = (b & 7) * 8 + (b >> 5), dq = (b >> 3) & 3;
  const int w = __builtin_amdgcn_readfirstlane(threadIdx.x >> 6), l = threadIdx.x & 63, g = l >> 4, hb = 9 * dq + w;
  const i32x4 dO = mkdesc(ws + WS_O, S_ * D_ * 2);
  u32x4 yp[16], wf[18];
  const auto yr = __builtin_amdgcn_make_buffer_rsrc((void*)(ws + WS_YP), 0, (unsigned)YP_BYTES, 0x00020000);
  const auto wr = __builtin_amdgcn_make_buffer_rsrc((void*)(ws + WS_WOT), 0, 36 * 18 * 1024, 0x00020000);
#pragma unroll
  for (int jj = 0; jj < YH; ++jj) yp[jj] = __builtin_amdgcn_raw_buffer_load_b128(yr, l * 16, ((tg * 36 + hb) * 16 + jj) * 1024, YLD);
  SCHED();
#pragma unroll
  for (int m = 0; m < 4; ++m) {
    const int fi = 4 * w + m, tb = fi >= 18, s = fi - 18 * tb;
    dma16(L0 + fi * 1024, (l & 15) * 1152 + g * 16, dO, (32 * tg + 16 * tb) * 1152 + 64 * s);
  }
  SCHED();
#pragma unroll
  for (int s = 0; s < 18; ++s) wf[s] = __builtin_amdgcn_raw_buffer_load_b128(wr, l * 16, (hb * 18 + s) * 1024, 0);
  SCHED();
  const size_t xo = (size_t)(32 * tg + (l & 15)) * D_ + 16 * hb + 8 * (g & 1) + 4 * (g >> 1);
  const u32x2 x0 = *(const u32x2*)(x + xo), x1 = *(const u32x2*)(x + xo + 16 * D_);
#pragma unroll
  for (int jj = YH; jj < 16; ++jj) yp[jj] = __builtin_amdgcn_raw_buffer_load_b128(yr, l * 16, ((tg * 36 + hb) * 16 + jj) * 1024, YLD);
  SCHED();
  asm volatile("s_waitcnt vmcnt(%0)" :: "i"(36 - YH) : "memory");
  BAR();
  f32x4 s0 = {}, s1 = {};
#pragma unroll
  for (int s = 0; s < 18; ++s) {
    s0 = mfma16(wf[s], lds_rd128(L0 + s * 1024 + l * 16), s0);
    s1 = mfma16(wf[s], lds_rd128(L0 + (18 + s) * 1024 + l * 16), s1);
  }
  f32x4 y0 = {}, y1 = {};
#pragma unroll
  for (int jj = 0; jj < 16; ++jj) {
    y0 += f32x4{bf_lo(yp[jj][0]), bf_hi(yp[jj][0]), bf_lo(yp[jj][1]), bf_hi(yp[jj][1])};
    y1 += f32x4{bf_lo(yp[jj][2]), bf_hi(yp[jj][2]), bf_lo(yp[jj][3]), bf_hi(yp[jj][3])};
  }
  const u32x2 r0 = {cvtpk(bf_lo(x0[0]) + s0[0] * silu_f(y0[0]), bf_hi(x0[0]) + s0[1] * silu_f(y0[1])),
                    cvtpk(bf_lo(x0[1]) + s0[2] * silu_f(y0[2]), bf_hi(x0[1]) + s0[3] * silu_f(y0[3]))};
  const u32x2 r1 = {cvtpk(bf_lo(x1[0]) + s1[0] * silu_f(y1[0]), bf_hi(x1[0]) + s1[1] * silu_f(y1[1])),
                    cvtpk(bf_lo(x1[1]) + s1[2] * silu_f(y1[2]), bf_hi(x1[1]) + s1[3] * silu_f(y1[3]))};
  *(u32x2*)(out + xo) = r0;
  *(u32x2*)(out + xo + 16 * D_) = r1;
}

extern "C" void kernel_launch(void* const* d_in, const int* in_sizes, int n_in, void* d_out, int out_size, void* d_ws,
                              size_t ws_size, hipStream_t stream) {
  const unsigned short* x = (const unsigned short*)d_in[0];
  const unsigned short* wqkv = (const unsigned short*)d_in[1];
  const unsigned short* wphi = (const unsigned short*)d_in[2];
  const unsigned short* wo = (const unsigned short*)d_in[3];
  const unsigned short* wg = (const unsigned short*)d_in[5];
  const unsigned short* wu = (const unsigned short*)d_in[6];
  const unsigned short* wd = (const unsigned short*)d_in[7];
  char* ws = (char*)d_ws;
  k_prep<<<256, 512, 0, stream>>>(x, wqkv, wo, wphi, ws);
  k_main<<<256, 512, 0, stream>>>(wg, wu, wd, ws);
  k_final<<<256, 576, 0, stream>>>(x, ws, (unsigned short*)d_out);
}
```

```cpp
#define JMAP (2 * (b & 7) + ((b >> 3) & 1))
#define TMAP (b >> 4)
#include <hip/hip_runtime.h>
#include <hip/hip_bf16.h>
#include <stdint.h>

typedef __attribute__((ext_vector_type(8))) short s16x8;
typedef __attribute__((ext_vector_type(4))) short s16x4;
typedef __attribute__((ext_vector_type(8))) __bf16 bf16x8;
typedef __attribute__((ext_vector_type(2))) __bf16 bf16x2;
typedef __attribute__((ext_vector_type(16))) float f32x16;
typedef __attribute__((ext_vector_type(4))) float f32x4;
typedef __attribute__((ext_vector_type(2))) float f32x2;
typedef __attribute__((ext_vector_type(4))) unsigned u32x4;
typedef __attribute__((ext_vector_type(2))) unsigned u32x2;
typedef __attribute__((ext_vector_type(4))) int i32x4;
#define LAS __attribute__((address_space(3)))
#define GAS __attribute__((address_space(1)))
#define DEV __device__ __forceinline__

template <int I> struct IC { static constexpr int v = I; };
template <int B, int E, class Fn> DEV void sfor(Fn&& fn) {
  if constexpr (B < E) { fn(IC<B>{}); sfor<B + 1, E>(fn); }
}

constexpr int S_ = 2048, D_ = 576, F_ = 4608;
constexpr size_t WS_XN = 0;
constexpr size_t WS_WOT = WS_XN + 64 * 36 * 1024;
constexpr size_t WS_KVP = WS_WOT + 36 * 18 * 1024;
constexpr size_t WS_O = WS_KVP + 32 * 16 * 3840;
constexpr size_t WS_CNT = WS_O + 2048 * 576 * 2;
constexpr size_t WS_KVF = WS_CNT + 32 * 256;
constexpr size_t WS_QI = WS_KVF + 16 * 4096;
constexpr size_t WS_YP = WS_QI + 16 * 6 * 24576;
constexpr size_t YP_BYTES = (size_t)16 * 64 * 36 * 1024;

DEV unsigned cvtpk(float lo, float hi) {
  f32x2 v = {lo, hi};
  return __builtin_bit_cast(unsigned, __builtin_convertvector(v, bf16x2));
}
DEV float bf_lo(unsigned u) { return __uint_as_float(u << 16); }
DEV float bf_hi(unsigned u) { return __uint_as_float(u & 0xffff0000u); }
DEV f32x16 mfma32(u32x4 a, u32x4 b, f32x16 c) {
  return __builtin_amdgcn_mfma_f32_32x32x16_bf16(__builtin_bit_cast(bf16x8, a), __builtin_bit_cast(bf16x8, b), c, 0, 0, 0);
}
DEV f32x4 mfma16(u32x4 a, u32x4 b, f32x4 c) {
  return __builtin_amdgcn_mfma_f32_16x16x32_bf16(__builtin_bit_cast(bf16x8, a), __builtin_bit_cast(bf16x8, b), c, 0, 0, 0);
}
DEV u32x2 trd(unsigned addr) {
  return __builtin_bit_cast(u32x2, __builtin_amdgcn_ds_read_tr16_b64_v4i16((LAS s16x4*)(uintptr_t)addr));
}
DEV u32x4 trfrag(unsigned a0, unsigned a1) {
  u32x2 lo = trd(a0), hi = trd(a1);
  u32x4 r = {lo[0], lo[1], hi[0], hi[1]};
  return r;
}
DEV u32x4 lds_rd128(unsigned a) { return *(LAS u32x4*)(uintptr_t)a; }
DEV u32x2 lds_rd64(unsigned a) { return *(LAS u32x2*)(uintptr_t)a; }
DEV void lds_wr128(unsigned a, u32x4 v) { *(LAS u32x4*)(uintptr_t)a = v; }
DEV void lds_wr64(unsigned a, u32x2 v) { *(LAS u32x2*)(uintptr_t)a = v; }
DEV i32x4 mkdesc(const void* p, unsigned bytes) {
  uint64_t a = (uint64_t)p;
  i32x4 d = {(int)__builtin_amdgcn_readfirstlane((unsigned)a), (int)__builtin_amdgcn_readfirstlane((unsigned)(a >> 32) & 0xffffu),
             (int)bytes, 0x00020000};
  return d;
}
DEV void dma16(unsigned lds, unsigned voff, i32x4 desc, unsigned soff) {
  asm volatile("s_mov_b32 m0, %0\n\ts_nop 0\n\tbuffer_load_dwordx4 %1, %2, %3 offen lds"
               :: "s"(lds), "v"(voff), "s"(desc), "s"(soff) : "memory");
}
template <unsigned OFF> DEV void dma16i(unsigned lds, unsigned voff, i32x4 desc, unsigned soff) {
  asm volatile("s_add_u32 m0, %0, %4\n\ts_nop 0\n\tbuffer_load_dwordx4 %1, %2, %3 offen lds"
               :: "s"(lds), "v"(voff), "s"(desc), "s"(soff), "i"(OFF) : "memory", "scc");
}
#define WAITV(n) asm volatile("s_waitcnt vmcnt(" #n ")" ::: "memory")
#define ST(k)
#define STR(k)
#define SCHED() __builtin_amdgcn_sched_barrier(0)
DEV unsigned opq(unsigned v) { asm volatile("" : "+v"(v)); return v; }
#define WAITL0() asm volatile("s_waitcnt lgkmcnt(0)" ::: "memory")
#define BAR() do { asm volatile("" ::: "memory"); __builtin_amdgcn_s_barrier(); asm volatile("" ::: "memory"); } while (0)
DEV float xhalf_max(float x) {
  auto t = __builtin_amdgcn_permlane32_swap(__float_as_uint(x), __float_as_uint(x), false, false);
  return fmaxf(__uint_as_float(t[0]), __uint_as_float(t[1]));
}
DEV float xhalf_sum(float x) {
  auto t = __builtin_amdgcn_permlane32_swap(__float_as_uint(x), __float_as_uint(x), false, false);
  return __uint_as_float(t[0]) + __uint_as_float(t[1]);
}
constexpr float LOG2E = 1.4426950408889634f;
DEV float silu_f(float g) { return g * __builtin_amdgcn_rcpf(1.0f + __builtin_amdgcn_exp2f(-LOG2E * g)); }

DEV void hedgehog(const float (&pv)[10], int h, u32x4& pos, u32x4& neg, u32x2& ext) {
  float mx = pv[0], mn = pv[0];
#pragma unroll
  for (int r = 1; r < 8; ++r) { mx = fmaxf(mx, pv[r]); mn = fminf(mn, pv[r]); }
  if (h == 0) { mx = fmaxf(mx, fmaxf(pv[8], pv[9])); mn = fminf(mn, fminf(pv[8], pv[9])); }
  mx = xhalf_max(mx); mn = -xhalf_max(-mn);
  float ep[10], en[10], sp = 0.f, sn = 0.f;
#pragma unroll
  for (int r = 0; r < 10; ++r) {
    ep[r] = __builtin_amdgcn_exp2f((pv[r] - mx) * LOG2E);
    en[r] = __builtin_amdgcn_exp2f((mn - pv[r]) * LOG2E);
    if (r >= 8) { ep[r] = h ? 0.f : ep[r]; en[r] = h ? 0.f : en[r]; }
    sp += ep[r]; sn += en[r];
  }
  float ip = __builtin_amdgcn_rcpf(xhalf_sum(sp)), in = __builtin_amdgcn_rcpf(xhalf_sum(sn));
#pragma unroll
  for (int i = 0; i < 4; ++i) {
    pos[i] = cvtpk(ep[2 * i] * ip, ep[2 * i + 1] * ip);
    neg[i] = cvtpk(en[2 * i] * in, en[2 * i + 1] * in);
  }
  ext[0] = cvtpk(ep[8] * ip, ep[9] * ip);
  ext[1] = cvtpk(en[8] * in, en[9] * in);
}

__global__ __launch_bounds__(512) void k_prep(const unsigned short* __restrict__ x, const unsigned short* __restrict__ wqkv,
                                              const unsigned short* __restrict__ wo,
                                              const unsigned short* __restrict__ wphi, char* __restrict__ ws) {
  const int b = blockIdx.x, w = threadIdx.x >> 6, l = threadIdx.x & 63;
  if (w < 2) {
    const int tok = 8 * b + 4 * w + (l >> 4), i = l & 15;
    u32x2 v[9];
#pragma unroll
    for (int u = 0; u < 9; ++u) v[u] = *(const u32x2*)(x + (size_t)tok * D_ + 4 * (i + 16 * u));
    float f[36], sum = 0.f;
#pragma unroll
    for (int u = 0; u < 9; ++u) {
      f[4 * u] = bf_lo(v[u][0]); f[4 * u + 1] = bf_hi(v[u][0]); f[4 * u + 2] = bf_lo(v[u][1]); f[4 * u + 3] = bf_hi(v[u][1]);
      sum += (f[4 * u] + f[4 * u + 1]) + (f[4 * u + 2] + f[4 * u + 3]);
    }
#pragma unroll
    for (int m = 1; m < 16; m <<= 1) sum += __shfl_xor(sum, m);
    const float mean = sum * (1.0f / D_);
    float sq = 0.f;
#pragma unroll
    for (int e = 0; e < 36; ++e) { f[e] -= mean; sq += f[e] * f[e]; }
#pragma unroll
    for (int m = 1; m < 16; m <<= 1) sq += __shfl_xor(sq, m);
    const float rs = 1.0f / sqrtf(sq * (1.0f / D_) + 1e-5f);
    char* dst = ws + WS_XN + (size_t)(tok >> 5) * 36864 + (tok & 31) * 16;
#pragma unroll
    for (int u = 0; u < 9; ++u) {
      const int c8 = i + 16 * u, c = c8 >> 1;
      u32x2 o = {cvtpk(f[4 * u] * rs, f[4 * u + 1] * rs), cvtpk(f[4 * u + 2] * rs, f[4 * u + 3] * rs)};
      *(u32x2*)(dst + (c >> 1) * 1024 + (c & 1) * 512 + (c8 & 1) * 8) = o;
    }
  } else if (w >= 5) {
    u32x4 v[3];
#pragma unroll
    for (int m = 0; m < 3; ++m) {
      const int id = 576 * b + (int)threadIdx.x - 320 + 192 * m, jj = id / 9216, g = (id % 9216) / 1536;
      const int c = min(id % 1536, 1439), k16 = c / 240, r = c % 240, seg = r / 80, pos = (r % 80) / 5, ch = r % 5;
      const int row = 96 * g + 16 * k16 + 8 * ((pos >> 1) & 1) + 4 * (pos & 1) + (pos >> 2);
      const int off = min(row * 3456 + seg * 1152 + ((72 * jj) & ~15) + 16 * ch, D_ * 3 * D_ * 2 - 16);
      v[m] = *(const u32x4*)((const char*)wqkv + off);
    }
#pragma unroll
    for (int m = 0; m < 3; ++m) {
      const int id = 576 * b + (int)threadIdx.x - 320 + 192 * m, g = (id % 9216) / 1536;
      if (!(id % 1536 >= 1440 && (g == 2 || g == 3))) *(u32x4*)(ws + WS_QI + (size_t)id * 16) = v[m];
    }
  } else {
    for (int fi = 3 * b + (w - 2); fi < 648 + 64; fi += 768) {
      unsigned short e[8];
      char* dst;
      if (fi < 648) {
        const int hb = fi / 18, s = fi % 18, i = l & 15, g = l >> 4, gi = i >> 2;
        const int d = 16 * hb + 8 * (gi & 1) + 4 * (gi >> 1) + (i & 3);
#pragma unroll
        for (int jj = 0; jj < 8; ++jj) e[jj] = wo[(size_t)(32 * s + 8 * g + jj) * D_ + d];
        dst = ws + WS_WOT + (size_t)fi * 1024;
      } else {
        const int q = fi - 648, head = q >> 1, s = q & 1, f = l & 31, h = l >> 5;
#pragma unroll
        for (int jj = 0; jj < 8; ++jj) {
          const int d = 16 * s + 8 * (jj >> 2) + 4 * h + (jj & 3) - 2 * (head & 1);
          const bool ok = f < 18 && d >= 0 && d < 18;
          const unsigned short t = wphi[head * 324 + (ok ? d * 18 + f : 0)];
          e[jj] = ok ? t : (unsigned short)0;
        }
        dst = ws + WS_QI + (size_t)((head >> 1) * 6 + 2 + (head & 1)) * 24576 + 23040;
        if (s) {
          *(u32x2*)(dst + 1024 + l * 8) = u32x2{e[0] | ((unsigned)e[1] << 16), e[2] | ((unsigned)e[3] << 16)};
          continue;
        }
      }
      u32x4 o = {e[0] | ((unsigned)e[1] << 16), e[2] | ((unsigned)e[3] << 16), e[4] | ((unsigned)e[5] << 16), e[6] | ((unsigned)e[7] << 16)};
      *(u32x4*)(dst + l * 16) = o;
    }
  }
  if (b == 0 && threadIdx.x < 32) *(unsigned*)(ws + WS_CNT + threadIdx.x * 256) = 0u;
}

constexpr unsigned SLOT = 24576, HALF = 3 * SLOT, SPARE = 6 * SLOT, LDS_MAIN = 163840;
#define RY 1
#define RO 8
#define PFC 1
#define PFW 1
#define NA 3
#define NB 6
#define NQ 6
#define YH 0
#define YLD 2
#define YAUX 16

__global__ __launch_bounds__(512, 2) void k_main(const unsigned short* __restrict__ wg, const unsigned short* __restrict__ wu,
                                                 const unsigned short* __restrict__ wd, char* __restrict__ ws) {
  __shared__ __attribute__((aligned(1024))) char lds[LDS_MAIN];
  const unsigned L0 = (unsigned)(uintptr_t)(LAS char*)lds;
  const int b = blockIdx.x, tid = threadIdx.x;
  const int j = JMAP, t = TMAP, jo = j & 1;
  const int w = __builtin_amdgcn_readfirstlane(tid >> 6), tg4 = w >> 1, p = w & 1;
  int l = tid & 63, h = l >> 5, g16 = (l >> 4) & 1, q4 = (l >> 2) & 3, pp = l & 3;
  const int tg = 4 * t + tg4;
  GAS unsigned* const cnt1 = (GAS unsigned*)(ws + WS_CNT + j * 256);
  GAS unsigned* const cnt2 = cnt1 + 1024;

  const i32x4 dW = mkdesc(ws, (unsigned)WS_YP), dA = mkdesc(p ? wu : wg, D_ * F_ * 2), dB = mkdesc(wd, D_ * F_ * 2);
  unsigned l16 = l * 16;
  const unsigned vA2 = (l >> 3) * 9216 + ((((l >> 1) & 3) ^ (((l >> 4) & 1) + 2 * p)) * 32) + (l & 1) * 16;
  unsigned vA1 = 0, vB = 0;
  auto pieceX = [&](int half, int m) {
    const int n = 9 * w + m, tq = w >> 1, s = 18 * half + n - 18 * tq;
    dma16(L0 + n * 1024, l16, dW, (unsigned)WS_XN + ((4 * t + tq) * 36 + s) * 1024);
  };
  auto pieceQ = [&](int g, int m) {
    dma16(L0 + (3 + g % 3) * SLOT + (w + 8 * m) * 1024, l16, dW, (unsigned)WS_QI + (j * 6 + g) * SLOT + (w + 8 * m) * 1024);
  };
  auto pieceA2 = [&](int v, int i, int m) {
    dma16(L0 + i * SLOT + (w + 8 * m) * 1024, vA2, dA,
          (96 * i + 16 * ((w >> 2) + 2 * m) + 8 * (tg4 & 1)) * 9216 + (288 * j + 32 * jo + 64 * v) * 2);
  };
  auto pieceA1 = [&](int i, int m) {
    dma16(L0 + i * SLOT + (w + 8 * m) * 1024, vA1, dA, (192 * i + 16 * (tg4 + 4 * m)) * 9216 + (288 * j + 256 * (1 - jo)) * 2);
  };
  const bool grp = w >= 4;
  const unsigned wl = L0 + w * 1024;
  const unsigned sA2 = (16 * (w >> 2) + 8 * (tg4 & 1)) * 9216 + (288 * j + 32 * jo) * 2;
  unsigned sB[9];
#pragma unroll
  for (int e = 0; e < 9; ++e) {
    const int it = 12 * (e / 3) + tg4 + 4 * (e % 3), dp = it >= 18;
    sB[e] = (288 * j + 16 * (it - 18 * dp)) * 1152 + 128 * dp;
  }
  bool timeout = false;
  auto spin16 = [&](GAS unsigned* c, unsigned seen) {
    unsigned spins = 0;
    while (__builtin_amdgcn_readfirstlane(seen) < 16u) {
      __builtin_amdgcn_s_sleep(2);
      seen = __hip_atomic_load(c, __ATOMIC_RELAXED, __HIP_MEMORY_SCOPE_AGENT);
      if (++spins > (1u << 22)) { timeout = true; break; }
    }
  };

  STR(15); ST(0);
  u32x4 X[36], wp0, wp1;
#pragma unroll
  for (int m = 0; m < 9; ++m) pieceX(0, m);
#pragma unroll
  for (int m = 0; m < 9; ++m) pieceQ(m / 3, m % 3);
  WAITV(3); BAR();
  const unsigned rx = opq(L0 + tg4 * 18432 + l16);
#pragma unroll
  for (int s = 0; s < 18; ++s) X[s] = lds_rd128(rx + s * 1024);
  WAITL0(); BAR();
  {
    uint64_t pc;
    asm volatile("s_getpc_b64 %0" : "=s"(pc));
    asm volatile("s_mov_b32 m0, %0\n\ts_nop 0\n\tglobal_load_lds_dword %1, %2"
                 :: "s"(L0 + SPARE + w * 256), "v"(min(tid, 279) * 128), "s"(pc) : "memory");
    const int id = t * 180 + tg4 * 45 + min(l, 44);
    asm volatile("s_mov_b32 m0, %0\n\ts_nop 0\n\tbuffer_load_dword %1, %2, %3 offen lds"
                 :: "s"(L0 + SPARE + 4096 + w * 256), "v"((id / 5) * 9216 + (id % 5) * 128), "s"(dA), "s"((576 * j) & ~127) : "memory");
    asm volatile("s_mov_b32 m0, %0\n\ts_nop 0\n\tbuffer_load_dword %1, %2, %3 offen lds"
                 :: "s"(L0 + SPARE + 2048 + w * 256), "v"((t * 162 + min(tid, 161)) * 128), "s"(dB), "s"(288 * j * 1152) : "memory");
  }
  ST(1);

  f32x16 aq = {}, ak = {}, av = {};
  {
    const unsigned rq = L0 + 3 * SLOT + (4 * q4 + 2 * h) * 80 + ((72 * j) & 15) + 32 * p + 32 * g16 + 8 * pp;
#define QADDR(n) (rb[((n) / 18) & 1] + (((n) % 18) / 3) * 3840 + ((n) % 3) * 1280)
    u32x4 a[NQ];
    unsigned rb[2] = {rq, rq};
    sfor<0, 108>([&](auto nn) {
      constexpr int n = decltype(nn)::v, g = n / 18, r = n % 18;
      if (r == 0) {
        if (g == 1 || g == 4) WAITV(9);
        if (g == 3) WAITV(3);
        if (g) BAR();
        rb[(g + 1) & 1] = opq(rq + ((g + 1) % 3) * SLOT);
        if (g == 3) {
#pragma unroll
          for (int s = 18; s < 36; ++s) X[s] = lds_rd128(rx + (s - 18) * 1024);
          const unsigned wa = L0 + (p ? 3 : 5) * SLOT + 23040;
          wp0 = lds_rd128(wa + l16);
          const u32x2 w1 = lds_rd64(wa + 1024 + l * 8);
          wp1 = u32x4{w1[0], w1[1], 0u, 0u};
        }
        if (g == 0 || g == 3) {
#pragma unroll
          for (int e = 0; e < NQ; ++e) a[e] = trfrag(QADDR(n + e), QADDR(n + e) + 80);
        }
        if (g == 3) { WAITL0(); BAR(); }
        SCHED();
      }
      if (g == 0 && r < 9) pieceX(1, r);
      if ((g == 1 || g == 2) && r < 3) pieceQ(g + 2, r);
      if (g == 3 && r < 3) pieceQ(5, r);
      if (g == 3 && r >= 3 && r < 12) pieceA2(0, (r - 3) / 3, r % 3);
      if (g >= 4 && r % 6 == 1) pieceA2(0, g - 1, r / 6);
      constexpr int s = n / 3;
      if (n % 3 == 0) aq = mfma32(a[n % NQ], X[s], aq);
      else if (n % 3 == 1) ak = mfma32(a[n % NQ], X[s], ak);
      else av = mfma32(X[s], a[n % NQ], av);
      if (n + NQ < 108 && (n + NQ < 54 || n >= 54)) a[n % NQ] = trfrag(QADDR(n + NQ), QADDR(n + NQ) + 80);
      if (n == 107 || n == 53) asm volatile("" : "+v"(aq), "+v"(ak), "+v"(av));
      SCHED();
    });
  }
  ST(2);
  constexpr unsigned PQ_OFF = 5 * SLOT + 8 * 3840;
  {
    auto phi = [&](const f32x16& c) {
      u32x4 b0 = {cvtpk(c[0], c[1]), cvtpk(c[2], c[3]), cvtpk(c[4], c[5]), cvtpk(c[6], c[7])};
      u32x4 b1 = {cvtpk(c[8], c[9]), cvtpk(c[10], c[11]), 0u, 0u};
      b0[0] = (p && !h) ? 0u : b0[0];
      b1[0] = h ? 0u : b1[0];
      b1[1] = (p && !h) ? b1[1] : 0u;
      f32x16 z = {};
      z = mfma32(wp0, b0, z);
      return mfma32(wp1, b1, z);
    };
    const f32x16 pq = phi(aq), pk = phi(ak);
#pragma unroll
    for (int i = 0; i < 5; ++i) *(LAS unsigned*)(uintptr_t)(L0 + PQ_OFF + i * 2048 + tid * 4) = cvtpk(pq[2 * i], pq[2 * i + 1]);
    float pv[10];
#pragma unroll
    for (int i = 0; i < 5; ++i) { const unsigned u = cvtpk(pk[2 * i], pk[2 * i + 1]); pv[2 * i] = bf_lo(u); pv[2 * i + 1] = bf_hi(u); }
    u32x4 pos, neg; u32x2 ext;
    hedgehog(pv, h, pos, neg, ext);
    BAR();
    const unsigned R = L0 + 5 * SLOT + w * 3840, tk = l & 31;
    lds_wr64(R + tk * 64 + 8 * h, u32x2{pos[0], pos[1]});
    lds_wr64(R + tk * 64 + 16 + 8 * h, u32x2{pos[2], pos[3]});
    lds_wr64(R + tk * 64 + 32 + 8 * h, u32x2{neg[0], neg[1]});
    lds_wr64(R + tk * 64 + 48 + 8 * h, u32x2{neg[2], neg[3]});
    lds_wr64(R + 2048 + l * 8, ext);
    asm volatile("" ::: "memory");
    f32x16 kv0 = {}, kv1 = {};
#pragma unroll
    for (int s = 0; s < 2; ++s) {
      u32x4 vb;
#pragma unroll
      for (int i = 0; i < 4; ++i) vb[i] = (tk == 20) ? 0x3f803f80u : cvtpk(av[8 * s + 2 * i], av[8 * s + 2 * i + 1]);
      const unsigned r0 = 16 * s + 4 * h + q4;
      kv0 = mfma32(trfrag(R + r0 * 64 + 32 * g16 + 8 * pp, R + (r0 + 8) * 64 + 32 * g16 + 8 * pp), vb, kv0);
      kv1 = mfma32(trfrag(R + 2048 + r0 * 8, R + 2048 + (r0 + 8) * 8), vb, kv1);
    }
    asm volatile("" ::: "memory");
    if (tk < 24) {
      const unsigned Rl = R + (h * 24 + tk) * 16;
#pragma unroll
      for (int qd = 0; qd < 4; ++qd)
        lds_wr128(Rl + qd * 768, __builtin_bit_cast(u32x4, f32x4{kv0[4 * qd], kv0[4 * qd + 1], kv0[4 * qd + 2], kv0[4 * qd + 3]}));
      lds_wr128(Rl + 3072, __builtin_bit_cast(u32x4, f32x4{kv1[0], kv1[1], kv1[2], kv1[3]}));
    }
    WAITL0(); WAITV(9); BAR();
    if (tid >= 272) {
      const int rem = tid - 272;
#pragma unroll
      for (int pq_ = 0; pq_ < 2; ++pq_) {
        f32x4 sum = {};
#pragma unroll
        for (int e = 0; e < 4; ++e) sum += __builtin_bit_cast(f32x4, lds_rd128(L0 + 5 * SLOT + (2 * e + pq_) * 3840 + rem * 16));
        __builtin_amdgcn_raw_buffer_store_b128(__builtin_bit_cast(u32x4, sum),
            __builtin_amdgcn_make_buffer_rsrc(ws + WS_KVP, 0, 32 * 16 * 3840, 0x00020000),
            ((2 * j + pq_) * 16 + t) * 3840 + rem * 16, 0, 16);
      }
    }
    WAITL0();
  }

  ST(3);
  u32x4 H[18], k0, k1, k2;
  {
    u32x4 a[NA], hh[9];
    auto act = [&](const f32x16& c) {
      u32x4 r;
#pragma unroll
      for (int i = 0; i < 4; ++i) r[i] = cvtpk(silu_f(c[2 * i]) * c[8 + 2 * i], silu_f(c[2 * i + 1]) * c[9 + 2 * i]);
      return r;
    };
    const unsigned rA2 = L0 + h * 2048 + g16 * 1024 + q4 * 128 + (((2 * p) ^ ((q4 >> 1) + 2 * g16)) * 32) + pp * 8;
    const auto kvp = __builtin_amdgcn_make_buffer_rsrc(ws + WS_KVP, 0, 32 * 16 * 3840, 0x00020000);
    const auto kvf = __builtin_amdgcn_make_buffer_rsrc(ws + WS_KVF + j * 4096, 0, 4096, 0x00020000);
#pragma unroll
    for (int e = 0; e < NA; ++e) { const unsigned ad = (rA2 ^ ((e & 1) * 32)) + (e >> 1) * 4096; a[e] = trfrag(ad, ad + 512); }
    f32x16 c0 = {}, c1 = {};
    unsigned pk[16];
    float e0, e1, u0, u1;
    auto opf = [](float x) { asm volatile("" : "+v"(x)); return x; };
    auto micro = [&](auto nn, auto vv) {
      constexpr int n = decltype(nn)::v, d = n / 8, k = n % 8, gi = (d / 4) * 8 + d % 4, v = decltype(vv)::v;
      if constexpr (n >= 0 && n < 64) {
        if (k == 0) e0 = opf(-LOG2E * bf_lo(pk[gi]));
        if (k == 1) { e0 = opf(__builtin_amdgcn_exp2f(e0)); e1 = opf(bf_hi(pk[gi])); }
        if (k == 2) { e0 = opf(1.0f + e0); u1 = opf(-LOG2E * e1); }
        if (k == 3) { u1 = opf(__builtin_amdgcn_exp2f(u1)); u0 = opf(bf_hi(pk[gi + 4])); }
        if (k == 4) { e0 = opf(__builtin_amdgcn_rcpf(e0)); u1 = opf(1.0f + u1); }
        if (k == 5) { u1 = opf(__builtin_amdgcn_rcpf(u1)); e1 = opf(e1 * u0); }
        if (k == 6) u0 = opf(bf_lo(pk[gi]) * bf_lo(pk[gi + 4]));
        if (k == 7) hh[2 * v + d / 4][d % 4] = opq(cvtpk(u0 * e0, e1 * u1));
      }
    };
    auto dunit = [&](auto vv) {
      constexpr int v = decltype(vv)::v;
      constexpr bool last = v == 3;
      unsigned rb[2][2], seen = 0;
      u32x4 kl = {}, kl2 = {};
      const unsigned sv = sA2 + 128 * v;
      rb[0][0] = opq(rA2); rb[0][1] = opq(rA2 ^ 32);
      sfor<0, 72>([&](auto nn) {
        constexpr int n = decltype(nn)::v, i = n / 12, r = n % 12;
        if (r == 0) {
          if (((i == 1 && v == 0) || (i == 0 && v == 2)) && grp) WAITV(0); else WAITV(9);
          BAR();
          if (!last) {
            if (i == 1 && v == 0 && tid == 0) __hip_atomic_fetch_add(cnt1, 1u, __ATOMIC_RELAXED, __HIP_MEMORY_SCOPE_AGENT);
            if (i == 0 && v == 2 && tid == 0) __hip_atomic_fetch_add(cnt2, 1u, __ATOMIC_RELAXED, __HIP_MEMORY_SCOPE_AGENT);
            if (v == 1 && grp) {
              if (i == 0) seen = __hip_atomic_load(cnt1, __ATOMIC_RELAXED, __HIP_MEMORY_SCOPE_AGENT);
              if (i == 2) {
                spin16(cnt1, seen);
                const int I = 30 * t + min((tid - 256) >> 3, 29), pq_ = I >= 240;
                const int o = ((2 * j + pq_) * 16 + (tid & 7)) * 3840 + (I - 240 * pq_) * 16;
                kl = __builtin_amdgcn_raw_buffer_load_b128(kvp, o, 0, 16);
                kl2 = __builtin_amdgcn_raw_buffer_load_b128(kvp, o, 8 * 3840, 16);
              }
              if (i == 4) {
                f32x4 sm = __builtin_bit_cast(f32x4, kl) + __builtin_bit_cast(f32x4, kl2);
#pragma unroll
                for (int e = 0; e < 4; ++e) {
                  sm[e] += __int_as_float(__builtin_amdgcn_update_dpp(0, __float_as_int(sm[e]), 0xB1, 0xf, 0xf, true));
                  sm[e] += __int_as_float(__builtin_amdgcn_update_dpp(0, __float_as_int(sm[e]), 0x4E, 0xf, 0xf, true));
                  sm[e] += __int_as_float(__builtin_amdgcn_update_dpp(0, __float_as_int(sm[e]), 0x141, 0xf, 0xf, true));
                }
                if (timeout) sm[0] = __uint_as_float(0x7fc00000u);
                if ((tid & 7) == 0 && tid < 496)
                  __builtin_amdgcn_raw_buffer_store_b64(u32x2{cvtpk(sm[0], sm[1]), cvtpk(sm[2], sm[3])}, kvf, (30 * t + ((tid - 256) >> 3)) * 8, 0, 16);
              }
            }
          }
          if (i < 5) { rb[(i + 1) & 1][0] = opq(rA2 + (i + 1) * SLOT); rb[(i + 1) & 1][1] = opq((rA2 ^ 32) + (i + 1) * SLOT); }
          else { rb[0][0] = opq(rA2); rb[0][1] = opq(rA2 ^ 32); }
          SCHED();
        }
        auto pieces = [&]() {
          sfor<0, 3>([&](auto mm) {
            constexpr int m = decltype(mm)::v;
            if (i == 0) dma16i<5 * SLOT + m * 8192>(wl, vA2, dA, sv + (480 + 32 * m) * 9216);
            else if (!last) dma16i<(i - 1) * SLOT + m * 8192>(wl, vA2, dA, sv + 128 + (96 * (i - 1) + 32 * m) * 9216);
            else if (i < 4) pieceA1(i - 1, m);
            else dma16i<(i - 1) * SLOT + m * 8192>(wl, vB, dB, sB[(i == 5) * 3 + m]);
          });
        };
        if (r == RY && grp) pieces();
        if (n < 2) { const f32x16 z = {}; if (n == 0) c0 = mfma32(a[0], X[0], z); else c1 = mfma32(a[1], X[0], z); }
        else if (n % 2 == 0) c0 = mfma32(a[n % NA], X[n / 2], c0);
        else c1 = mfma32(a[n % NA], X[n / 2], c1);
        constexpr int q = n + NA;
        const unsigned ad = rb[(q / 12) & 1][q & 1] + ((q % 12) >> 1) * 4096;
        if (!(last && q >= 72)) a[n % NA] = trfrag(ad, ad + 512);
        if constexpr (v > 0) micro(IC<n - 2>{}, IC<v - 1>{});
        if (r == RO && !grp) pieces();
        SCHED();
      });
#pragma unroll
      for (int e = 0; e < 8; ++e) { pk[e] = opq(cvtpk(c0[2 * e], c0[2 * e + 1])); pk[8 + e] = opq(cvtpk(c1[2 * e], c1[2 * e + 1])); }
      SCHED();
    };
    dunit(IC<0>{}); dunit(IC<1>{}); dunit(IC<2>{});
    l = opq(__builtin_amdgcn_mbcnt_hi(~0u, __builtin_amdgcn_mbcnt_lo(~0u, 0u)));
    vA1 = (l >> 2) * 9216 + ((((l >> 1) & 1) ^ p) * 32) + (l & 1) * 16;
    vB = (8 * p + 4 * (l >> 5) + ((l >> 2) & 3)) * 1152 + 64 * ((l >> 4) & 1) + 16 * (l & 3);
    dunit(IC<3>{});
    l = opq(__builtin_amdgcn_mbcnt_hi(~0u, __builtin_amdgcn_mbcnt_lo(~0u, 0u)));
    h = l >> 5; g16 = (l >> 4) & 1; q4 = (l >> 2) & 3; pp = l & 3; l16 = l * 16;
    const unsigned rA1 = L0 + g16 * 1024 + h * 512 + q4 * 64 + (p ^ g16) * 32 + pp * 8;
    u32x2 kc[5];
    {
      const unsigned r0 = opq(rA1), r2 = opq(rA1 + 2 * SLOT);
      unsigned seen = __hip_atomic_load(cnt2, __ATOMIC_RELAXED, __HIP_MEMORY_SCOPE_AGENT);
      c0 = f32x16{};
      sfor<0, 36>([&](auto nn) {
        constexpr int s = decltype(nn)::v;
        if (s == 0) {
#pragma unroll
          for (int e = 0; e < NA; ++e) a[e] = trfrag(r0 + e * 2048, r0 + e * 2048 + 256);
          WAITV(9); BAR();
        }
        if (s == 12) {
          WAITV(9); BAR();
          spin16(cnt2, seen);
          const unsigned ka = (p * 240 + h * 24 + min(l & 31, 23)) * 8;
#pragma unroll
          for (int e = 0; e < 5; ++e) kc[e] = __builtin_amdgcn_raw_buffer_load_b64(kvf, ka + e * 384, 0, 16);
          SCHED();
        }
        if ((s == RY && grp) || (s == RO && !grp))
          sfor<0, 3>([&](auto mm) { constexpr int m = decltype(mm)::v; dma16i<5 * SLOT + m * 8192>(wl, vB, dB, sB[6 + m]); });
        c0 = mfma32(a[s % NA], X[s], c0);
        constexpr int q = s + NA;
        if (q < 36) { const unsigned ad = q < 24 ? r0 + q * 2048 : r2 + (q - 24) * 2048; a[s % NA] = trfrag(ad, ad + 256); }
        micro(IC<2 * s - 4>{}, IC<3>{}); micro(IC<2 * s - 3>{}, IC<3>{});
        SCHED();
      });
      hh[8] = act(c0);
    }
    ST(4);
    BAR();
    ST(5);
#pragma unroll
    for (int e = 0; e < 8; ++e) lds_wr128(L0 + (tg4 * 18 + 2 * jo + 4 * (e >> 1) + 2 * p + (e & 1)) * 1024 + l16, hh[e]);
    lds_wr128(L0 + (tg4 * 18 + 16 * (1 - jo) + p) * 1024 + l16, hh[8]);
    WAITL0(); BAR();
#pragma unroll
    for (int s = 0; s < 18; ++s) H[s] = lds_rd128(L0 + tg4 * 18432 + l16 + s * 1024);
    k0 = u32x4{kc[0][0], kc[0][1], kc[1][0], kc[1][1]}; k1 = u32x4{kc[2][0], kc[2][1], kc[3][0], kc[3][1]};
    k2 = u32x4{h ? 0u : kc[4][0], h ? 0u : kc[4][1], 0u, 0u};
    WAITL0();
    WAITV(0); BAR();
    sfor<0, 9>([&](auto mm) { constexpr int m = decltype(mm)::v; dma16i<(m / 3) * SLOT + (m % 3) * 8192>(wl, vB, dB, sB[m] + 256); });
  }
  ST(7);
  {
    unsigned cur = L0 + HALF, oth = L0;
    const unsigned rB = h * 512 + p * 256 + q4 * 64 + g16 * 32 + pp * 8;
    const auto yr = __builtin_amdgcn_make_buffer_rsrc(ws + WS_YP, 0, (unsigned)YP_BYTES, 0x00020000);
    const unsigned ybase = ((tg * 36 + 2 * p) * 16 + j) * 1024;
    u32x4 a[NB];
#pragma unroll
    for (int e = 0; e < NB; ++e) a[e] = trfrag(cur + rB + e * 2048, cur + rB + e * 2048 + 1024);
    auto ocomp = [&]() {
    float pv[10];
#pragma unroll
    for (int i = 0; i < 5; ++i) {
      const unsigned u = *(LAS unsigned*)(uintptr_t)(L0 + PQ_OFF + i * 2048 + w * 256 + l * 4);
      pv[2 * i] = bf_lo(u); pv[2 * i + 1] = bf_hi(u);
    }
    u32x4 pos, neg; u32x2 ext;
    hedgehog(pv, h, pos, neg, ext);
    f32x16 on = {};
    on = mfma32(k0, pos, on); on = mfma32(k1, neg, on); on = mfma32(k2, u32x4{ext[0], ext[1], 0u, 0u}, on);
    const float inv = timeout ? __uint_as_float(0x7fc00000u) : __builtin_amdgcn_rcpf(xhalf_sum(h ? on[8] : 0.f) + 1e-6f);
    char* op = ws + WS_O + (size_t)(32 * tg + (l & 31)) * 1152 + (36 * j + 16 * p + 4 * h) * 2;
    const u32x2 o0 = {cvtpk(on[0] * inv, on[1] * inv), cvtpk(on[2] * inv, on[3] * inv)};
    const u32x2 o1 = {cvtpk(on[4] * inv, on[5] * inv), cvtpk(on[6] * inv, on[7] * inv)};
    const u32x2 o2 = {cvtpk(on[8] * inv, on[9] * inv), cvtpk(on[10] * inv, on[11] * inv)};
    if (p && !h) *(unsigned*)(op + 4) = o0[1]; else *(u32x2*)op = o0;
    *(u32x2*)(op + 16) = o1;
    if (!h) { if (p) *(u32x2*)(op + 32) = o2; else *(unsigned*)(op + 32) = o2[0]; }
      };
    auto period = [&](auto last_, int P) {
      constexpr bool last = decltype(last_)::v;
      const unsigned rc = opq(cur + rB), rc2 = opq(cur + rB + 2 * SLOT), rn = opq(oth + rB);
      const unsigned wc = cur + w * 1024, wo_ = oth + w * 1024, sp = 256 * P;
      f32x16 acc = {};
      sfor<0, (last ? 18 : 36)>([&](auto nn) {
        constexpr int n = decltype(nn)::v, i = n / 12, r = n % 12;
        if (r == 0) {
          if (last) { if (i == 0) { WAITV(6); BAR(); } }
          else if (P == 3) { if (i == 0) WAITV(13); else if (i == 1) WAITV(10); else WAITV(7); BAR(); }
          else if (i > 0 || P > 0) { WAITV(11); BAR(); }
          if (!last && i == 0 && P == (grp ? 1 : 2)) ocomp();
          SCHED();
        }
        if (!last && ((r == RY && grp) || (r == RO && !grp)) && P < 3 && (i > 0 || P > 0))
          sfor<0, 3>([&](auto mm) {
            constexpr int m = decltype(mm)::v;
            if (i == 0) dma16i<2 * SLOT + m * 8192>(wo_, vB, dB, sB[6 + m] + 256 + sp);
            else dma16i<(i ? i - 1 : 0) * SLOT + m * 8192>(wc, vB, dB, sB[(i ? i - 1 : 0) * 3 + m] + 512 + sp);
          });
        if (n % 18 == 0) { const f32x16 z = {}; acc = mfma32(a[n % NB], H[0], z); }
        else acc = mfma32(a[n % NB], H[n % 18], acc);
        constexpr int q = n + NB;
        const unsigned ad = q < 24 ? rc + q * 2048 : q < 36 ? rc2 + (q - 24) * 2048 : rn + (q - 36) * 2048;
        if (!(last && q >= 18)) a[n % NB] = trfrag(ad, ad + 1024);
        SCHED();
        if (n % 18 == 17) {
          const unsigned so = ybase + (2 * P + n / 18) * 65536;
#pragma unroll
          for (int e = 0; e < 2; ++e) {
            const auto s0 = __builtin_amdgcn_permlane16_swap(cvtpk(acc[8 * e], acc[8 * e + 1]), cvtpk(acc[8 * e + 4], acc[8 * e + 5]), false, false);
            const auto s1 = __builtin_amdgcn_permlane16_swap(cvtpk(acc[8 * e + 2], acc[8 * e + 3]), cvtpk(acc[8 * e + 6], acc[8 * e + 7]), false, false);
            const u32x4 y = {s0[0], s1[0], s0[1], s1[1]};
            __builtin_amdgcn_raw_buffer_store_b128(y, yr, l16, so + e * 16384, YAUX);
          }
          SCHED();
        }
      });
      const unsigned tmp = cur; cur = oth; oth = tmp;
    };
#pragma unroll 1
    for (int P = 0; P < 4; ++P) period(IC<0>{}, P);
    period(IC<1>{}, 4);
  }
  ST(8);
  ST(9); ST(13); STR(14);
}

__global__ __launch_bounds__(576, 3) void k_final(const unsigned short* __restrict__ x, const char* __restrict__ ws,
                                                  unsigned short* __restrict__ out) {
  __shared__ __attribute__((aligned(1024))) char lds[36864];
  const unsigned L0 = (unsigned)(uintptr_t)(LAS char*)lds;
  const int b = blockIdx.x, tg = (b & 7) * 8 + (b >> 5), dq = (b >> 3) & 3;
  const int w = __builtin_amdgcn_readfirstlane(threadIdx.x >> 6), l = threadIdx.x & 63, g = l >> 4, hb = 9 * dq + w;
  const i32x4 dO = mkdesc(ws + WS_O, S_ * D_ * 2);
  u32x4 yp[16], wf[18];
  const auto yr = __builtin_amdgcn_make_buffer_rsrc((void*)(ws + WS_YP), 0, (unsigned)YP_BYTES, 0x00020000);
  const auto wr = __builtin_amdgcn_make_buffer_rsrc((void*)(ws + WS_WOT), 0, 36 * 18 * 1024, 0x00020000);
#pragma unroll
  for (int jj = 0; jj < YH; ++jj) yp[jj] = __builtin_amdgcn_raw_buffer_load_b128(yr, l * 16, ((tg * 36 + hb) * 16 + jj) * 1024, YLD);
  SCHED();
#pragma unroll
  for (int m = 0; m < 4; ++m) {
    const int fi = 4 * w + m, tb = fi >= 18, s = fi - 18 * tb;
    dma16(L0 + fi * 1024, (l & 15) * 1152 + g * 16, dO, (32 * tg + 16 * tb) * 1152 + 64 * s);
  }
  SCHED();
#pragma unroll
  for (int s = 0; s < 18; ++s) wf[s] = __builtin_amdgcn_raw_buffer_load_b128(wr, l * 16, (hb * 18 + s) * 1024, 0);
  SCHED();
  const size_t xo = (size_t)(32 * tg + (l & 15)) * D_ + 16 * hb + 8 * (g & 1) + 4 * (g >> 1);
  const u32x2 x0 = *(const u32x2*)(x + xo), x1 = *(const u32x2*)(x + xo + 16 * D_);
#pragma unroll
  for (int jj = YH; jj < 16; ++jj) yp[jj] = __builtin_amdgcn_raw_buffer_load_b128(yr, l * 16, ((tg * 36 + hb) * 16 + jj) * 1024, YLD);
  SCHED();
  asm volatile("s_waitcnt vmcnt(%0)" :: "i"(36 - YH) : "memory");
  BAR();
  f32x4 s0 = {}, s1 = {};
#pragma unroll
  for (int s = 0; s < 18; ++s) {
    s0 = mfma16(wf[s], lds_rd128(L0 + s * 1024 + l * 16), s0);
    s1 = mfma16(wf[s], lds_rd128(L0 + (18 + s) * 1024 + l * 16), s1);
  }
  f32x4 y0 = {}, y1 = {};
#pragma unroll
  for (int jj = 0; jj < 16; ++jj) {
    y0 += f32x4{bf_lo(yp[jj][0]), bf_hi(yp[jj][0]), bf_lo(yp[jj][1]), bf_hi(yp[jj][1])};
    y1 += f32x4{bf_lo(yp[jj][2]), bf_hi(yp[jj][2]), bf_lo(yp[jj][3]), bf_hi(yp[jj][3])};
  }
  const u32x2 r0 = {cvtpk(bf_lo(x0[0]) + s0[0] * silu_f(y0[0]), bf_hi(x0[0]) + s0[1] * silu_f(y0[1])),
                    cvtpk(bf_lo(x0[1]) + s0[2] * silu_f(y0[2]), bf_hi(x0[1]) + s0[3] * silu_f(y0[3]))};
  const u32x2 r1 = {cvtpk(bf_lo(x1[0]) + s1[0] * silu_f(y1[0]), bf_hi(x1[0]) + s1[1] * silu_f(y1[1])),
                    cvtpk(bf_lo(x1[1]) + s1[2] * silu_f(y1[2]), bf_hi(x1[1]) + s1[3] * silu_f(y1[3]))};
  *(u32x2*)(out + xo) = r0;
  *(u32x2*)(out + xo + 16 * D_) = r1;
}

extern "C" void kernel_launch(void* const* d_in, const int* in_sizes, int n_in, void* d_out, int out_size, void* d_ws,
                              size_t ws_size, hipStream_t stream) {
  const unsigned short* x = (const unsigned short*)d_in[0];
  const unsigned short* wqkv = (const unsigned short*)d_in[1];
  const unsigned short* wphi = (const unsigned short*)d_in[2];
  const unsigned short* wo = (const unsigned short*)d_in[3];
  const unsigned short* wg = (const unsigned short*)d_in[5];
  const unsigned short* wu = (const unsigned short*)d_in[6];
  const unsigned short* wd = (const unsigned short*)d_in[7];
  char* ws = (char*)d_ws;
  k_prep<<<256, 512, 0, stream>>>(x, wqkv, wo, wphi, ws);
  k_main<<<256, 512, 0, stream>>>(wg, wu, wd, ws);
  k_final<<<256, 576, 0, stream>>>(x, ws, (unsigned short*)d_out);
}
```

```cpp
#define JMAP (2 * (b & 7) + ((b >> 3) & 1))
#define TMAP (b >> 4)
#include <hip/hip_runtime.h>
#include <hip/hip_bf16.h>
#include <stdint.h>

typedef __attribute__((ext_vector_type(8))) short s16x8;
typedef __attribute__((ext_vector_type(4))) short s16x4;
typedef __attribute__((ext_vector_type(8))) __bf16 bf16x8;
typedef __attribute__((ext_vector_type(2))) __bf16 bf16x2;
typedef __attribute__((ext_vector_type(16))) float f32x16;
typedef __attribute__((ext_vector_type(4))) float f32x4;
typedef __attribute__((ext_vector_type(2))) float f32x2;
typedef __attribute__((ext_vector_type(4))) unsigned u32x4;
typedef __attribute__((ext_vector_type(2))) unsigned u32x2;
typedef __attribute__((ext_vector_type(4))) int i32x4;
#define LAS __attribute__((address_space(3)))
#define GAS __attribute__((address_space(1)))
#define DEV __device__ __forceinline__

template <int I> struct IC { static constexpr int v = I; };
template <int B, int E, class Fn> DEV void sfor(Fn&& fn) {
  if constexpr (B < E) { fn(IC<B>{}); sfor<B + 1, E>(fn); }
}

constexpr int S_ = 2048, D_ = 576, F_ = 4608;
constexpr size_t WS_XN = 0;
constexpr size_t WS_WOT = WS_XN + 64 * 36 * 1024;
constexpr size_t WS_KVP = WS_WOT + 36 * 18 * 1024;
constexpr size_t WS_O = WS_KVP + 32 * 16 * 3840;
constexpr size_t WS_CNT = WS_O + 2048 * 576 * 2;
constexpr size_t WS_KVF = WS_CNT + 32 * 256;
constexpr size_t WS_QI = WS_KVF + 16 * 4096;
constexpr size_t WS_YP = WS_QI + 16 * 6 * 24576;
constexpr size_t YP_BYTES = (size_t)16 * 64 * 36 * 1024;

DEV unsigned cvtpk(float lo, float hi) {
  f32x2 v = {lo, hi};
  return __builtin_bit_cast(unsigned, __builtin_convertvector(v, bf16x2));
}
DEV float bf_lo(unsigned u) { return __uint_as_float(u << 16); }
DEV float bf_hi(unsigned u) { return __uint_as_float(u & 0xffff0000u); }
DEV f32x16 mfma32(u32x4 a, u32x4 b, f32x16 c) {
  return __builtin_amdgcn_mfma_f32_32x32x16_bf16(__builtin_bit_cast(bf16x8, a), __builtin_bit_cast(bf16x8, b), c, 0, 0, 0);
}
DEV f32x4 mfma16(u32x4 a, u32x4 b, f32x4 c) {
  return __builtin_amdgcn_mfma_f32_16x16x32_bf16(__builtin_bit_cast(bf16x8, a), __builtin_bit_cast(bf16x8, b), c, 0, 0, 0);
}
DEV u32x2 trd(unsigned addr) {
  return __builtin_bit_cast(u32x2, __builtin_amdgcn_ds_read_tr16_b64_v4i16((LAS s16x4*)(uintptr_t)addr));
}
DEV u32x4 trfrag(unsigned a0, unsigned a1) {
  u32x2 lo = trd(a0), hi = trd(a1);
  u32x4 r = {lo[0], lo[1], hi[0], hi[1]};
  return r;
}
DEV u32x4 lds_rd128(unsigned a) { return *(LAS u32x4*)(uintptr_t)a; }
DEV u32x2 lds_rd64(unsigned a) { return *(LAS u32x2*)(uintptr_t)a; }
DEV void lds_wr128(unsigned a, u32x4 v) { *(LAS u32x4*)(uintptr_t)a = v; }
DEV void lds_wr64(unsigned a, u32x2 v) { *(LAS u32x2*)(uintptr_t)a = v; }
DEV i32x4 mkdesc(const void* p, unsigned bytes) {
  uint64_t a = (uint64_t)p;
  i32x4 d = {(int)__builtin_amdgcn_readfirstlane((unsigned)a), (int)__builtin_amdgcn_readfirstlane((unsigned)(a >> 32) & 0xffffu),
             (int)bytes, 0x00020000};
  return d;
}
DEV void dma16(unsigned lds, unsigned voff, i32x4 desc, unsigned soff) {
  asm volatile("s_mov_b32 m0, %0\n\ts_nop 0\n\tbuffer_load_dwordx4 %1, %2, %3 offen lds"
               :: "s"(lds), "v"(voff), "s"(desc), "s"(soff) : "memory");
}
template <unsigned OFF> DEV void dma16i(unsigned lds, unsigned voff, i32x4 desc, unsigned soff) {
  asm volatile("s_add_u32 m0, %0, %4\n\ts_nop 0\n\tbuffer_load_dwordx4 %1, %2, %3 offen lds"
               :: "s"(lds), "v"(voff), "s"(desc), "s"(soff), "i"(OFF) : "memory", "scc");
}
#define WAITV(n) asm volatile("s_waitcnt vmcnt(" #n ")" ::: "memory")
#define ST(k)
#define STR(k)
#define SCHED() __builtin_amdgcn_sched_barrier(0)
DEV unsigned opq(unsigned v) { asm volatile("" : "+v"(v)); return v; }
#define WAITL0() asm volatile("s_waitcnt lgkmcnt(0)" ::: "memory")
#define BAR() do { asm volatile("" ::: "memory"); __builtin_amdgcn_s_barrier(); asm volatile("" ::: "memory"); } while (0)
DEV float xhalf_max(float x) {
  auto t = __builtin_amdgcn_permlane32_swap(__float_as_uint(x), __float_as_uint(x), false, false);
  return fmaxf(__uint_as_float(t[0]), __uint_as_float(t[1]));
}
DEV float xhalf_sum(float x) {
  auto t = __builtin_amdgcn_permlane32_swap(__float_as_uint(x), __float_as_uint(x), false, false);
  return __uint_as_float(t[0]) + __uint_as_float(t[1]);
}
constexpr float LOG2E = 1.4426950408889634f;
DEV float silu_f(float g) { return g * __builtin_amdgcn_rcpf(1.0f + __builtin_amdgcn_exp2f(-LOG2E * g)); }

DEV void hedgehog(const float (&pv)[10], int h, u32x4& pos, u32x4& neg, u32x2& ext) {
  float mx = pv[0], mn = pv[0];
#pragma unroll
  for (int r = 1; r < 8; ++r) { mx = fmaxf(mx, pv[r]); mn = fminf(mn, pv[r]); }
  if (h == 0) { mx = fmaxf(mx, fmaxf(pv[8], pv[9])); mn = fminf(mn, fminf(pv[8], pv[9])); }
  mx = xhalf_max(mx); mn = -xhalf_max(-mn);
  float ep[10], en[10], sp = 0.f, sn = 0.f;
#pragma unroll
  for (int r = 0; r < 10; ++r) {
    ep[r] = __builtin_amdgcn_exp2f((pv[r] - mx) * LOG2E);
    en[r] = __builtin_amdgcn_exp2f((mn - pv[r]) * LOG2E);
    if (r >= 8) { ep[r] = h ? 0.f : ep[r]; en[r] = h ? 0.f : en[r]; }
    sp += ep[r]; sn += en[r];
  }
  float ip = __builtin_amdgcn_rcpf(xhalf_sum(sp)), in = __builtin_amdgcn_rcpf(xhalf_sum(sn));
#pragma unroll
  for (int i = 0; i < 4; ++i) {
    pos[i] = cvtpk(ep[2 * i] * ip, ep[2 * i + 1] * ip);
    neg[i] = cvtpk(en[2 * i] * in, en[2 * i + 1] * in);
  }
  ext[0] = cvtpk(ep[8] * ip, ep[9] * ip);
  ext[1] = cvtpk(en[8] * in, en[9] * in);
}

__global__ __launch_bounds__(512) void k_prep(const unsigned short* __restrict__ x, const unsigned short* __restrict__ wqkv,
                                              const unsigned short* __restrict__ wo,
                                              const unsigned short* __restrict__ wphi, const unsigned* __restrict__ wg,
                                              const unsigned* __restrict__ wu, const unsigned* __restrict__ wd,
                                              char* __restrict__ ws) {
  const int b = blockIdx.x, w = threadIdx.x >> 6, l = threadIdx.x & 63;
  unsigned touch;
  {
    const int id = 486 * b + min((int)threadIdx.x, 485), m = id / 41472;
    const unsigned* src = (m == 0 ? wg : m == 1 ? wu : wd) + (id - m * 41472) * 32;
    touch = *src;
  }
  unsigned touch2 = 0;
  if (b < 8 && threadIdx.x < 280) {
    uint64_t pc;
    asm volatile("s_getpc_b64 %0" : "=s"(pc));
    touch2 = *(const GAS unsigned*)((pc & ~3ull) + 4096 + threadIdx.x * 128);
  }
  if (w < 2) {
    const int tok = 8 * b + 4 * w + (l >> 4), i = l & 15;
    u32x2 v[9];
#pragma unroll
    for (int u = 0; u < 9; ++u) v[u] = *(const u32x2*)(x + (size_t)tok * D_ + 4 * (i + 16 * u));
    float f[36], sum = 0.f;
#pragma unroll
    for (int u = 0; u < 9; ++u) {
      f[4 * u] = bf_lo(v[u][0]); f[4 * u + 1] = bf_hi(v[u][0]); f[4 * u + 2] = bf_lo(v[u][1]); f[4 * u + 3] = bf_hi(v[u][1]);
      sum += (f[4 * u] + f[4 * u + 1]) + (f[4 * u + 2] + f[4 * u + 3]);
    }
#pragma unroll
    for (int m = 1; m < 16; m <<= 1) sum += __shfl_xor(sum, m);
    const float mean = sum * (1.0f / D_);
    float sq = 0.f;
#pragma unroll
    for (int e = 0; e < 36; ++e) { f[e] -= mean; sq += f[e] * f[e]; }
#pragma unroll
    for (int m = 1; m < 16; m <<= 1) sq += __shfl_xor(sq, m);
    const float rs = 1.0f / sqrtf(sq * (1.0f / D_) + 1e-5f);
    char* dst = ws + WS_XN + (size_t)(tok >> 5) * 36864 + (tok & 31) * 16;
#pragma unroll
    for (int u = 0; u < 9; ++u) {
      const int c8 = i + 16 * u, c = c8 >> 1;
      u32x2 o = {cvtpk(f[4 * u] * rs, f[4 * u + 1] * rs), cvtpk(f[4 * u + 2] * rs, f[4 * u + 3] * rs)};
      *(u32x2*)(dst + (c >> 1) * 1024 + (c & 1) * 512 + (c8 & 1) * 8) = o;
    }
  } else if (w >= 5) {
    u32x4 v[3];
#pragma unroll
    for (int m = 0; m < 3; ++m) {
      const int id = 576 * b + (int)threadIdx.x - 320 + 192 * m, jj = id / 9216, g = (id % 9216) / 1536;
      const int c = min(id % 1536, 1439), k16 = c / 240, r = c % 240, seg = r / 80, pos = (r % 80) / 5, ch = r % 5;
      const int row = 96 * g + 16 * k16 + 8 * ((pos >> 1) & 1) + 4 * (pos & 1) + (pos >> 2);
      const int off = min(row * 3456 + seg * 1152 + ((72 * jj) & ~15) + 16 * ch, D_ * 3 * D_ * 2 - 16);
      v[m] = *(const u32x4*)((const char*)wqkv + off);
    }
#pragma unroll
    for (int m = 0; m < 3; ++m) {
      const int id = 576 * b + (int)threadIdx.x - 320 + 192 * m, g = (id % 9216) / 1536;
      if (!(id % 1536 >= 1440 && (g == 2 || g == 3))) *(u32x4*)(ws + WS_QI + (size_t)id * 16) = v[m];
    }
  } else {
    for (int fi = 3 * b + (w - 2); fi < 648 + 64; fi += 768) {
      unsigned short e[8];
      char* dst;
      if (fi < 648) {
        const int hb = fi / 18, s = fi % 18, i = l & 15, g = l >> 4, gi = i >> 2;
        const int d = 16 * hb + 8 * (gi & 1) + 4 * (gi >> 1) + (i & 3);
#pragma unroll
        for (int jj = 0; jj < 8; ++jj) e[jj] = wo[(size_t)(32 * s + 8 * g + jj) * D_ + d];
        dst = ws + WS_WOT + (size_t)fi * 1024;
      } else {
        const int q = fi - 648, head = q >> 1, s = q & 1, f = l & 31, h = l >> 5;
#pragma unroll
        for (int jj = 0; jj < 8; ++jj) {
          const int d = 16 * s + 8 * (jj >> 2) + 4 * h + (jj & 3) - 2 * (head & 1);
          const bool ok = f < 18 && d >= 0 && d < 18;
          const unsigned short t = wphi[head * 324 + (ok ? d * 18 + f : 0)];
          e[jj] = ok ? t : (unsigned short)0;
        }
        dst = ws + WS_QI + (size_t)((head >> 1) * 6 + 2 + (head & 1)) * 24576 + 23040;
        if (s) {
          *(u32x2*)(dst + 1024 + l * 8) = u32x2{e[0] | ((unsigned)e[1] << 16), e[2] | ((unsigned)e[3] << 16)};
          continue;
        }
      }
      u32x4 o = {e[0] | ((unsigned)e[1] << 16), e[2] | ((unsigned)e[3] << 16), e[4] | ((unsigned)e[5] << 16), e[6] | ((unsigned)e[7] << 16)};
      *(u32x4*)(dst + l * 16) = o;
    }
  }
  if (b == 0 && threadIdx.x < 32) *(unsigned*)(ws + WS_CNT + threadIdx.x * 256) = 0u;
  asm volatile("" :: "v"(touch), "v"(touch2));
}

constexpr unsigned SLOT = 24576, HALF = 3 * SLOT, SPARE = 6 * SLOT, LDS_MAIN = 163840;
#define RY 1
#define RO 8
#define PFC 1
#define PFW 1
#define NA 3
#define NB 6
#define NQ 6
#define YH 0
#define YLD 2
#define YAUX 16

__global__ __launch_bounds__(512, 2) void k_main(const unsigned short* __restrict__ wg, const unsigned short* __restrict__ wu,
                                                 const unsigned short* __restrict__ wd, char* __restrict__ ws) {
  __shared__ __attribute__((aligned(1024))) char lds[LDS_MAIN];
  const unsigned L0 = (unsigned)(uintptr_t)(LAS char*)lds;
  const int b = blockIdx.x, tid = threadIdx.x;
  const int j = JMAP, t = TMAP, jo = j & 1;
  const int w = __builtin_amdgcn_readfirstlane(tid >> 6), tg4 = w >> 1, p = w & 1;
  int l = tid & 63, h = l >> 5, g16 = (l >> 4) & 1, q4 = (l >> 2) & 3, pp = l & 3;
  const int tg = 4 * t + tg4;
  GAS unsigned* const cnt1 = (GAS unsigned*)(ws + WS_CNT + j * 256);
  GAS unsigned* const cnt2 = cnt1 + 1024;

  const i32x4 dW = mkdesc(ws, (unsigned)WS_YP), dA = mkdesc(p ? wu : wg, D_ * F_ * 2), dB = mkdesc(wd, D_ * F_ * 2);
  unsigned l16 = l * 16;
  const unsigned vA2 = (l >> 3) * 9216 + ((((l >> 1) & 3) ^ (((l >> 4) & 1) + 2 * p)) * 32) + (l & 1) * 16;
  unsigned vA1 = 0, vB = 0;
  auto pieceX = [&](int half, int m) {
    const int n = 9 * w + m, tq = w >> 1, s = 18 * half + n - 18 * tq;
    dma16(L0 + n * 1024, l16, dW, (unsigned)WS_XN + ((4 * t + tq) * 36 + s) * 1024);
  };
  auto pieceQ = [&](int g, int m) {
    dma16(L0 + (3 + g % 3) * SLOT + (w + 8 * m) * 1024, l16, dW, (unsigned)WS_QI + (j * 6 + g) * SLOT + (w + 8 * m) * 1024);
  };
  auto pieceA2 = [&](int v, int i, int m) {
    dma16(L0 + i * SLOT + (w + 8 * m) * 1024, vA2, dA,
          (96 * i + 16 * ((w >> 2) + 2 * m) + 8 * (tg4 & 1)) * 9216 + (288 * j + 32 * jo + 64 * v) * 2);
  };
  auto pieceA1 = [&](int i, int m) {
    dma16(L0 + i * SLOT + (w + 8 * m) * 1024, vA1, dA, (192 * i + 16 * (tg4 + 4 * m)) * 9216 + (288 * j + 256 * (1 - jo)) * 2);
  };
  const bool grp = w >= 4;
  const unsigned wl = L0 + w * 1024;
  const unsigned sA2 = (16 * (w >> 2) + 8 * (tg4 & 1)) * 9216 + (288 * j + 32 * jo) * 2;
  unsigned sB[9];
#pragma unroll
  for (int e = 0; e < 9; ++e) {
    const int it = 12 * (e / 3) + tg4 + 4 * (e % 3), dp = it >= 18;
    sB[e] = (288 * j + 16 * (it - 18 * dp)) * 1152 + 128 * dp;
  }
  bool timeout = false;
  auto spin16 = [&](GAS unsigned* c, unsigned seen) {
    unsigned spins = 0;
    while (__builtin_amdgcn_readfirstlane(seen) < 16u) {
      __builtin_amdgcn_s_sleep(2);
      seen = __hip_atomic_load(c, __ATOMIC_RELAXED, __HIP_MEMORY_SCOPE_AGENT);
      if (++spins > (1u << 22)) { timeout = true; break; }
    }
  };

  STR(15); ST(0);
  u32x4 X[36], wp0, wp1;
#pragma unroll
  for (int m = 0; m < 9; ++m) pieceX(0, m);
#pragma unroll
  for (int m = 0; m < 9; ++m) pieceQ(m / 3, m % 3);
  WAITV(3); BAR();
  const unsigned rx = opq(L0 + tg4 * 18432 + l16);
#pragma unroll
  for (int s = 0; s < 18; ++s) X[s] = lds_rd128(rx + s * 1024);
  WAITL0(); BAR();
  {
    uint64_t pc;
    asm volatile("s_getpc_b64 %0" : "=s"(pc));
    asm volatile("s_mov_b32 m0, %0\n\ts_nop 0\n\tglobal_load_lds_dword %1, %2"
                 :: "s"(L0 + SPARE + w * 256), "v"(min(tid, 239) * 128), "s"(pc) : "memory");
    const int id = t * 180 + tg4 * 45 + min(l, 44);
    asm volatile("s_mov_b32 m0, %0\n\ts_nop 0\n\tbuffer_load_dword %1, %2, %3 offen lds"
                 :: "s"(L0 + SPARE + 4096 + w * 256), "v"((id / 5) * 9216 + (id % 5) * 128), "s"(dA), "s"((576 * j) & ~127) : "memory");
    asm volatile("s_mov_b32 m0, %0\n\ts_nop 0\n\tbuffer_load_dword %1, %2, %3 offen lds"
                 :: "s"(L0 + SPARE + 2048 + w * 256), "v"((t * 162 + min(tid, 161)) * 128), "s"(dB), "s"(288 * j * 1152) : "memory");
  }
  ST(1);

  f32x16 aq = {}, ak = {}, av = {};
  {
    const unsigned rq = L0 + 3 * SLOT + (4 * q4 + 2 * h) * 80 + ((72 * j) & 15) + 32 * p + 32 * g16 + 8 * pp;
#define QADDR(n) (rb[((n) / 18) & 1] + (((n) % 18) / 3) * 3840 + ((n) % 3) * 1280)
    u32x4 a[NQ];
    unsigned rb[2] = {rq, rq};
    sfor<0, 108>([&](auto nn) {
      constexpr int n = decltype(nn)::v, g = n / 18, r = n % 18;
      if (r == 0) {
        if (g == 1 || g == 4) WAITV(9);
        if (g == 3) WAITV(3);
        if (g) BAR();
        rb[(g + 1) & 1] = opq(rq + ((g + 1) % 3) * SLOT);
        if (g == 3) {
#pragma unroll
          for (int s = 18; s < 36; ++s) X[s] = lds_rd128(rx + (s - 18) * 1024);
          const unsigned wa = L0 + (p ? 3 : 5) * SLOT + 23040;
          wp0 = lds_rd128(wa + l16);
          const u32x2 w1 = lds_rd64(wa + 1024 + l * 8);
          wp1 = u32x4{w1[0], w1[1], 0u, 0u};
        }
        if (g == 0 || g == 3) {
#pragma unroll
          for (int e = 0; e < NQ; ++e) a[e] = trfrag(QADDR(n + e), QADDR(n + e) + 80);
        }
        if (g == 3) { WAITL0(); BAR(); }
        SCHED();
      }
      if (g == 0 && r < 9) pieceX(1, r);
      if ((g == 1 || g == 2) && r < 3) pieceQ(g + 2, r);
      if (g == 3 && r < 3) pieceQ(5, r);
      if (g == 3 && r >= 3 && r < 12) pieceA2(0, (r - 3) / 3, r % 3);
      if (g >= 4 && r % 6 == 1) pieceA2(0, g - 1, r / 6);
      constexpr int s = n / 3;
      if (n % 3 == 0) aq = mfma32(a[n % NQ], X[s], aq);
      else if (n % 3 == 1) ak = mfma32(a[n % NQ], X[s], ak);
      else av = mfma32(X[s], a[n % NQ], av);
      if (n + NQ < 108 && (n + NQ < 54 || n >= 54)) a[n % NQ] = trfrag(QADDR(n + NQ), QADDR(n + NQ) + 80);
      if (n == 107 || n == 53) asm volatile("" : "+v"(aq), "+v"(ak), "+v"(av));
      SCHED();
    });
  }
  ST(2);
  constexpr unsigned PQ_OFF = 5 * SLOT + 8 * 3840;
  {
    auto phi = [&](const f32x16& c) {
      u32x4 b0 = {cvtpk(c[0], c[1]), cvtpk(c[2], c[3]), cvtpk(c[4], c[5]), cvtpk(c[6], c[7])};
      u32x4 b1 = {cvtpk(c[8], c[9]), cvtpk(c[10], c[11]), 0u, 0u};
      b0[0] = (p && !h) ? 0u : b0[0];
      b1[0] = h ? 0u : b1[0];
      b1[1] = (p && !h) ? b1[1] : 0u;
      f32x16 z = {};
      z = mfma32(wp0, b0, z);
      return mfma32(wp1, b1, z);
    };
    const f32x16 pq = phi(aq), pk = phi(ak);
#pragma unroll
    for (int i = 0; i < 5; ++i) *(LAS unsigned*)(uintptr_t)(L0 + PQ_OFF + i * 2048 + tid * 4) = cvtpk(pq[2 * i], pq[2 * i + 1]);
    float pv[10];
#pragma unroll
    for (int i = 0; i < 5; ++i) { const unsigned u = cvtpk(pk[2 * i], pk[2 * i + 1]); pv[2 * i] = bf_lo(u); pv[2 * i + 1] = bf_hi(u); }
    u32x4 pos, neg; u32x2 ext;
    hedgehog(pv, h, pos, neg, ext);
    BAR();
    const unsigned R = L0 + 5 * SLOT + w * 3840, tk = l & 31;
    lds_wr64(R + tk * 64 + 8 * h, u32x2{pos[0], pos[1]});
    lds_wr64(R + tk * 64 + 16 + 8 * h, u32x2{pos[2], pos[3]});
    lds_wr64(R + tk * 64 + 32 + 8 * h, u32x2{neg[0], neg[1]});
    lds_wr64(R + tk * 64 + 48 + 8 * h, u32x2{neg[2], neg[3]});
    lds_wr64(R + 2048 + l * 8, ext);
    asm volatile("" ::: "memory");
    f32x16 kv0 = {}, kv1 = {};
#pragma unroll
    for (int s = 0; s < 2; ++s) {
      u32x4 vb;
#pragma unroll
      for (int i = 0; i < 4; ++i) vb[i] = (tk == 20) ? 0x3f803f80u : cvtpk(av[8 * s + 2 * i], av[8 * s + 2 * i + 1]);
      const unsigned r0 = 16 * s + 4 * h + q4;
      kv0 = mfma32(trfrag(R + r0 * 64 + 32 * g16 + 8 * pp, R + (r0 + 8) * 64 + 32 * g16 + 8 * pp), vb, kv0);
      kv1 = mfma32(trfrag(R + 2048 + r0 * 8, R + 2048 + (r0 + 8) * 8), vb, kv1);
    }
    asm volatile("" ::: "memory");
    if (tk < 24) {
      const unsigned Rl = R + (h * 24 + tk) * 16;
#pragma unroll
      for (int qd = 0; qd < 4; ++qd)
        lds_wr128(Rl + qd * 768, __builtin_bit_cast(u32x4, f32x4{kv0[4 * qd], kv0[4 * qd + 1], kv0[4 * qd + 2], kv0[4 * qd + 3]}));
      lds_wr128(Rl + 3072, __builtin_bit_cast(u32x4, f32x4{kv1[0], kv1[1], kv1[2], kv1[3]}));
    }
    WAITL0(); WAITV(9); BAR();
    if (tid >= 272) {
      const int rem = tid - 272;
#pragma unroll
      for (int pq_ = 0; pq_ < 2; ++pq_) {
        f32x4 sum = {};
#pragma unroll
        for (int e = 0; e < 4; ++e) sum += __builtin_bit_cast(f32x4, lds_rd128(L0 + 5 * SLOT + (2 * e + pq_) * 3840 + rem * 16));
        __builtin_amdgcn_raw_buffer_store_b128(__builtin_bit_cast(u32x4, sum),
            __builtin_amdgcn_make_buffer_rsrc(ws + WS_KVP, 0, 32 * 16 * 3840, 0x00020000),
            ((2 * j + pq_) * 16 + t) * 3840 + rem * 16, 0, 16);
      }
    }
    WAITL0();
  }

  ST(3);
  u32x4 H[18], k0, k1, k2;
  {
    u32x4 a[NA], hh[9];
    auto act = [&](const f32x16& c) {
      u32x4 r;
#pragma unroll
      for (int i = 0; i < 4; ++i) r[i] = cvtpk(silu_f(c[2 * i]) * c[8 + 2 * i], silu_f(c[2 * i + 1]) * c[9 + 2 * i]);
      return r;
    };
    const unsigned rA2 = L0 + h * 2048 + g16 * 1024 + q4 * 128 + (((2 * p) ^ ((q4 >> 1) + 2 * g16)) * 32) + pp * 8;
    const auto kvp = __builtin_amdgcn_make_buffer_rsrc(ws + WS_KVP, 0, 32 * 16 * 3840, 0x00020000);
    const auto kvf = __builtin_amdgcn_make_buffer_rsrc(ws + WS_KVF + j * 4096, 0, 4096, 0x00020000);
#pragma unroll
    for (int e = 0; e < NA; ++e) { const unsigned ad = (rA2 ^ ((e & 1) * 32)) + (e >> 1) * 4096; a[e] = trfrag(ad, ad + 512); }
    f32x16 c0 = {}, c1 = {};
    unsigned pk[16];
    float e0, e1, u0, u1;
    auto opf = [](float x) { asm volatile("" : "+v"(x)); return x; };
    auto micro = [&](auto nn, auto vv) {
      constexpr int n = decltype(nn)::v, d = n / 8, k = n % 8, gi = (d / 4) * 8 + d % 4, v = decltype(vv)::v;
      if constexpr (n >= 0 && n < 64) {
        if (k == 0) e0 = opf(-LOG2E * bf_lo(pk[gi]));
        if (k == 1) { e0 = opf(__builtin_amdgcn_exp2f(e0)); e1 = opf(bf_hi(pk[gi])); }
        if (k == 2) { e0 = opf(1.0f + e0); u1 = opf(-LOG2E * e1); }
        if (k == 3) { u1 = opf(__builtin_amdgcn_exp2f(u1)); u0 = opf(bf_hi(pk[gi + 4])); }
        if (k == 4) { e0 = opf(__builtin_amdgcn_rcpf(e0)); u1 = opf(1.0f + u1); }
        if (k == 5) { u1 = opf(__builtin_amdgcn_rcpf(u1)); e1 = opf(e1 * u0); }
        if (k == 6) u0 = opf(bf_lo(pk[gi]) * bf_lo(pk[gi + 4]));
        if (k == 7) hh[2 * v + d / 4][d % 4] = opq(cvtpk(u0 * e0, e1 * u1));
      }
    };
    auto dunit = [&](auto vv) {
      constexpr int v = decltype(vv)::v;
      constexpr bool last = v == 3;
      unsigned rb[2][2], seen = 0;
      u32x4 kl = {}, kl2 = {};
      const unsigned sv = sA2 + 128 * v;
      rb[0][0] = opq(rA2); rb[0][1] = opq(rA2 ^ 32);
      sfor<0, 72>([&](auto nn) {
        constexpr int n = decltype(nn)::v, i = n / 12, r = n % 12;
        if (r == 0) {
          if (((i == 1 && v == 0) || (i == 0 && v == 2)) && grp) WAITV(0); else WAITV(9);
          BAR();
          if (!last) {
            if (i == 1 && v == 0 && tid == 0) __hip_atomic_fetch_add(cnt1, 1u, __ATOMIC_RELAXED, __HIP_MEMORY_SCOPE_AGENT);
            if (i == 0 && v == 2 && tid == 0) __hip_atomic_fetch_add(cnt2, 1u, __ATOMIC_RELAXED, __HIP_MEMORY_SCOPE_AGENT);
            if (v == 1 && grp) {
              if (i == 0) seen = __hip_atomic_load(cnt1, __ATOMIC_RELAXED, __HIP_MEMORY_SCOPE_AGENT);
              if (i == 2) {
                spin16(cnt1, seen);
                const int I = 30 * t + min((tid - 256) >> 3, 29), pq_ = I >= 240;
                const int o = ((2 * j + pq_) * 16 + (tid & 7)) * 3840 + (I - 240 * pq_) * 16;
                kl = __builtin_amdgcn_raw_buffer_load_b128(kvp, o, 0, 16);
                kl2 = __builtin_amdgcn_raw_buffer_load_b128(kvp, o, 8 * 3840, 16);
              }
              if (i == 4) {
                f32x4 sm = __builtin_bit_cast(f32x4, kl) + __builtin_bit_cast(f32x4, kl2);
#pragma unroll
                for (int e = 0; e < 4; ++e) {
                  sm[e] += __int_as_float(__builtin_amdgcn_update_dpp(0, __float_as_int(sm[e]), 0xB1, 0xf, 0xf, true));
                  sm[e] += __int_as_float(__builtin_amdgcn_update_dpp(0, __float_as_int(sm[e]), 0x4E, 0xf, 0xf, true));
                  sm[e] += __int_as_float(__builtin_amdgcn_update_dpp(0, __float_as_int(sm[e]), 0x141, 0xf, 0xf, true));
                }
                if (timeout) sm[0] = __uint_as_float(0x7fc00000u);
                if ((tid & 7) == 0 && tid < 496)
                  __builtin_amdgcn_raw_buffer_store_b64(u32x2{cvtpk(sm[0], sm[1]), cvtpk(sm[2], sm[3])}, kvf, (30 * t + ((tid - 256) >> 3)) * 8, 0, 16);
              }
            }
          }
          if (i < 5) { rb[(i + 1) & 1][0] = opq(rA2 + (i + 1) * SLOT); rb[(i + 1) & 1][1] = opq((rA2 ^ 32) + (i + 1) * SLOT); }
          else { rb[0][0] = opq(rA2); rb[0][1] = opq(rA2 ^ 32); }
          SCHED();
        }
        auto pieces = [&]() {
          sfor<0, 3>([&](auto mm) {
            constexpr int m = decltype(mm)::v;
            if (i == 0) dma16i<5 * SLOT + m * 8192>(wl, vA2, dA, sv + (480 + 32 * m) * 9216);
            else if (!last) dma16i<(i - 1) * SLOT + m * 8192>(wl, vA2, dA, sv + 128 + (96 * (i - 1) + 32 * m) * 9216);
            else if (i < 4) pieceA1(i - 1, m);
            else dma16i<(i - 1) * SLOT + m * 8192>(wl, vB, dB, sB[(i == 5) * 3 + m]);
          });
        };
        if (r == RY && grp) pieces();
        if (n < 2) { const f32x16 z = {}; if (n == 0) c0 = mfma32(a[0], X[0], z); else c1 = mfma32(a[1], X[0], z); }
        else if (n % 2 == 0) c0 = mfma32(a[n % NA], X[n / 2], c0);
        else c1 = mfma32(a[n % NA], X[n / 2], c1);
        constexpr int q = n + NA;
        const unsigned ad = rb[(q / 12) & 1][q & 1] + ((q % 12) >> 1) * 4096;
        if (!(last && q >= 72)) a[n % NA] = trfrag(ad, ad + 512);
        if constexpr (v > 0) micro(IC<n - 2>{}, IC<v - 1>{});
        if (r == RO && !grp) pieces();
        SCHED();
      });
#pragma unroll
      for (int e = 0; e < 8; ++e) { pk[e] = opq(cvtpk(c0[2 * e], c0[2 * e + 1])); pk[8 + e] = opq(cvtpk(c1[2 * e], c1[2 * e + 1])); }
      SCHED();
    };
    dunit(IC<0>{}); dunit(IC<1>{}); dunit(IC<2>{});
    l = opq(__builtin_amdgcn_mbcnt_hi(~0u, __builtin_amdgcn_mbcnt_lo(~0u, 0u)));
    vA1 = (l >> 2) * 9216 + ((((l >> 1) & 1) ^ p) * 32) + (l & 1) * 16;
    vB = (8 * p + 4 * (l >> 5) + ((l >> 2) & 3)) * 1152 + 64 * ((l >> 4) & 1) + 16 * (l & 3);
    dunit(IC<3>{});
    l = opq(__builtin_amdgcn_mbcnt_hi(~0u, __builtin_amdgcn_mbcnt_lo(~0u, 0u)));
    h = l >> 5; g16 = (l >> 4) & 1; q4 = (l >> 2) & 3; pp = l & 3; l16 = l * 16;
    const unsigned rA1 = L0 + g16 * 1024 + h * 512 + q4 * 64 + (p ^ g16) * 32 + pp * 8;
    u32x2 kc[5];
    {
      const unsigned r0 = opq(rA1), r2 = opq(rA1 + 2 * SLOT);
      unsigned seen = __hip_atomic_load(cnt2, __ATOMIC_RELAXED, __HIP_MEMORY_SCOPE_AGENT);
      c0 = f32x16{};
      sfor<0, 36>([&](auto nn) {
        constexpr int s = decltype(nn)::v;
        if (s == 0) {
#pragma unroll
          for (int e = 0; e < NA; ++e) a[e] = trfrag(r0 + e * 2048, r0 + e * 2048 + 256);
          WAITV(9); BAR();
        }
        if (s == 12) {
          WAITV(9); BAR();
          spin16(cnt2, seen);
          const unsigned ka = (p * 240 + h * 24 + min(l & 31, 23)) * 8;
#pragma unroll
          for (int e = 0; e < 5; ++e) kc[e] = __builtin_amdgcn_raw_buffer_load_b64(kvf, ka + e * 384, 0, 16);
          SCHED();
        }
        if ((s == RY && grp) || (s == RO && !grp))
          sfor<0, 3>([&](auto mm) { constexpr int m = decltype(mm)::v; dma16i<5 * SLOT + m * 8192>(wl, vB, dB, sB[6 + m]); });
        c0 = mfma32(a[s % NA], X[s], c0);
        constexpr int q = s + NA;
        if (q < 36) { const unsigned ad = q < 24 ? r0 + q * 2048 : r2 + (q - 24) * 2048; a[s % NA] = trfrag(ad, ad + 256); }
        micro(IC<2 * s - 4>{}, IC<3>{}); micro(IC<2 * s - 3>{}, IC<3>{});
        SCHED();
      });
      hh[8] = act(c0);
    }
    ST(4);
    BAR();
    ST(5);
#pragma unroll
    for (int e = 0; e < 8; ++e) lds_wr128(L0 + (tg4 * 18 + 2 * jo + 4 * (e >> 1) + 2 * p + (e & 1)) * 1024 + l16, hh[e]);
    lds_wr128(L0 + (tg4 * 18 + 16 * (1 - jo) + p) * 1024 + l16, hh[8]);
    WAITL0(); BAR();
#pragma unroll
    for (int s = 0; s < 18; ++s) H[s] = lds_rd128(L0 + tg4 * 18432 + l16 + s * 1024);
    k0 = u32x4{kc[0][0], kc[0][1], kc[1][0], kc[1][1]}; k1 = u32x4{kc[2][0], kc[2][1], kc[3][0], kc[3][1]};
    k2 = u32x4{h ? 0u : kc[4][0], h ? 0u : kc[4][1], 0u, 0u};
    WAITL0();
    WAITV(0); BAR();
    sfor<0, 9>([&](auto mm) { constexpr int m = decltype(mm)::v; dma16i<(m / 3) * SLOT + (m % 3) * 8192>(wl, vB, dB, sB[m] + 256); });
  }
  ST(7);
  {
    unsigned cur = L0 + HALF, oth = L0;
    const unsigned rB = h * 512 + p * 256 + q4 * 64 + g16 * 32 + pp * 8;
    const auto yr = __builtin_amdgcn_make_buffer_rsrc(ws + WS_YP, 0, (unsigned)YP_BYTES, 0x00020000);
    const unsigned ybase = ((tg * 36 + 2 * p) * 16 + j) * 1024;
    u32x4 a[NB];
#pragma unroll
    for (int e = 0; e < NB; ++e) a[e] = trfrag(cur + rB + e * 2048, cur + rB + e * 2048 + 1024);
    auto ocomp = [&]() {
    float pv[10];
#pragma unroll
    for (int i = 0; i < 5; ++i) {
      const unsigned u = *(LAS unsigned*)(uintptr_t)(L0 + PQ_OFF + i * 2048 + w * 256 + l * 4);
      pv[2 * i] = bf_lo(u); pv[2 * i + 1] = bf_hi(u);
    }
    u32x4 pos, neg; u32x2 ext;
    hedgehog(pv, h, pos, neg, ext);
    f32x16 on = {};
    on = mfma32(k0, pos, on); on = mfma32(k1, neg, on); on = mfma32(k2, u32x4{ext[0], ext[1], 0u, 0u}, on);
    const float inv = timeout ? __uint_as_float(0x7fc00000u) : __builtin_amdgcn_rcpf(xhalf_sum(h ? on[8] : 0.f) + 1e-6f);
    char* op = ws + WS_O + (size_t)(32 * tg + (l & 31)) * 1152 + (36 * j + 16 * p + 4 * h) * 2;
    const u32x2 o0 = {cvtpk(on[0] * inv, on[1] * inv), cvtpk(on[2] * inv, on[3] * inv)};
    const u32x2 o1 = {cvtpk(on[4] * inv, on[5] * inv), cvtpk(on[6] * inv, on[7] * inv)};
    const u32x2 o2 = {cvtpk(on[8] * inv, on[9] * inv), cvtpk(on[10] * inv, on[11] * inv)};
    if (p && !h) *(unsigned*)(op + 4) = o0[1]; else *(u32x2*)op = o0;
    *(u32x2*)(op + 16) = o1;
    if (!h) { if (p) *(u32x2*)(op + 32) = o2; else *(unsigned*)(op + 32) = o2[0]; }
      };
    auto period = [&](auto last_, int P) {
      constexpr bool last = decltype(last_)::v;
      const unsigned rc = opq(cur + rB), rc2 = opq(cur + rB + 2 * SLOT), rn = opq(oth + rB);
      const unsigned wc = cur + w * 1024, wo_ = oth + w * 1024, sp = 256 * P;
      f32x16 acc = {};
      sfor<0, (last ? 18 : 36)>([&](auto nn) {
        constexpr int n = decltype(nn)::v, i = n / 12, r = n % 12;
        if (r == 0) {
          if (last) { if (i == 0) { WAITV(6); BAR(); } }
          else if (P == 3) { if (i == 0) WAITV(13); else if (i == 1) WAITV(10); else WAITV(7); BAR(); }
          else if (i > 0 || P > 0) { WAITV(11); BAR(); }
          if (!last && i == 0 && P == (grp ? 1 : 2)) ocomp();
          SCHED();
        }
        if (!last && ((r == RY && grp) || (r == RO && !grp)) && P < 3 && (i > 0 || P > 0))
          sfor<0, 3>([&](auto mm) {
            constexpr int m = decltype(mm)::v;
            if (i == 0) dma16i<2 * SLOT + m * 8192>(wo_, vB, dB, sB[6 + m] + 256 + sp);
            else dma16i<(i ? i - 1 : 0) * SLOT + m * 8192>(wc, vB, dB, sB[(i ? i - 1 : 0) * 3 + m] + 512 + sp);
          });
        if (n % 18 == 0) { const f32x16 z = {}; acc = mfma32(a[n % NB], H[0], z); }
        else acc = mfma32(a[n % NB], H[n % 18], acc);
        constexpr int q = n + NB;
        const unsigned ad = q < 24 ? rc + q * 2048 : q < 36 ? rc2 + (q - 24) * 2048 : rn + (q - 36) * 2048;
        if (!(last && q >= 18)) a[n % NB] = trfrag(ad, ad + 1024);
        SCHED();
        if (n % 18 == 17) {
          const unsigned so = ybase + (2 * P + n / 18) * 65536;
#pragma unroll
          for (int e = 0; e < 2; ++e) {
            const auto s0 = __builtin_amdgcn_permlane16_swap(cvtpk(acc[8 * e], acc[8 * e + 1]), cvtpk(acc[8 * e + 4], acc[8 * e + 5]), false, false);
            const auto s1 = __builtin_amdgcn_permlane16_swap(cvtpk(acc[8 * e + 2], acc[8 * e + 3]), cvtpk(acc[8 * e + 6], acc[8 * e + 7]), false, false);
            const u32x4 y = {s0[0], s1[0], s0[1], s1[1]};
            __builtin_amdgcn_raw_buffer_store_b128(y, yr, l16, so + e * 16384, YAUX);
          }
          SCHED();
        }
      });
      const unsigned tmp = cur; cur = oth; oth = tmp;
    };
#pragma unroll 1
    for (int P = 0; P < 4; ++P) period(IC<0>{}, P);
    period(IC<1>{}, 4);
  }
  ST(8);
  ST(9); ST(13); STR(14);
}

__global__ __launch_bounds__(576, 3) void k_final(const unsigned short* __restrict__ x, const char* __restrict__ ws,
                                                  unsigned short* __restrict__ out) {
  __shared__ __attribute__((aligned(1024))) char lds[36864];
  const unsigned L0 = (unsigned)(uintptr_t)(LAS char*)lds;
  const int b = blockIdx.x, tg = (b & 7) * 8 + (b >> 5), dq = (b >> 3) & 3;
  const int w = __builtin_amdgcn_readfirstlane(threadIdx.x >> 6), l = threadIdx.x & 63, g = l >> 4, hb = 9 * dq + w;
  const i32x4 dO = mkdesc(ws + WS_O, S_ * D_ * 2);
  u32x4 yp[16], wf[18];
  const auto yr = __builtin_amdgcn_make_buffer_rsrc((void*)(ws + WS_YP), 0, (unsigned)YP_BYTES, 0x00020000);
  const auto wr = __builtin_amdgcn_make_buffer_rsrc((void*)(ws + WS_WOT), 0, 36 * 18 * 1024, 0x00020000);
#pragma unroll
  for (int jj = 0; jj < YH; ++jj) yp[jj] = __builtin_amdgcn_raw_buffer_load_b128(yr, l * 16, ((tg * 36 + hb) * 16 + jj) * 1024, YLD);
  SCHED();
#pragma unroll
  for (int m = 0; m < 4; ++m) {
    const int fi = 4 * w + m, tb = fi >= 18, s = fi - 18 * tb;
    dma16(L0 + fi * 1024, (l & 15) * 1152 + g * 16, dO, (32 * tg + 16 * tb) * 1152 + 64 * s);
  }
  SCHED();
#pragma unroll
  for (int s = 0; s < 18; ++s) wf[s] = __builtin_amdgcn_raw_buffer_load_b128(wr, l * 16, (hb * 18 + s) * 1024, 0);
  SCHED();
  const size_t xo = (size_t)(32 * tg + (l & 15)) * D_ + 16 * hb + 8 * (g & 1) + 4 * (g >> 1);
  const u32x2 x0 = *(const u32x2*)(x + xo), x1 = *(const u32x2*)(x + xo + 16 * D_);
#pragma unroll
  for (int jj = YH; jj < 16; ++jj) yp[jj] = __builtin_amdgcn_raw_buffer_load_b128(yr, l * 16, ((tg * 36 + hb) * 16 + jj) * 1024, YLD);
  SCHED();
  asm volatile("s_waitcnt vmcnt(%0)" :: "i"(36 - YH) : "memory");
  BAR();
  f32x4 s0 = {}, s1 = {};
#pragma unroll
  for (int s = 0; s < 18; ++s) {
    s0 = mfma16(wf[s], lds_rd128(L0 + s * 1024 + l * 16), s0);
    s1 = mfma16(wf[s], lds_rd128(L0 + (18 + s) * 1024 + l * 16), s1);
  }
  f32x4 y0 = {}, y1 = {};
#pragma unroll
  for (int jj = 0; jj < 16; ++jj) {
    y0 += f32x4{bf_lo(yp[jj][0]), bf_hi(yp[jj][0]), bf_lo(yp[jj][1]), bf_hi(yp[jj][1])};
    y1 += f32x4{bf_lo(yp[jj][2]), bf_hi(yp[jj][2]), bf_lo(yp[jj][3]), bf_hi(yp[jj][3])};
  }
  const u32x2 r0 = {cvtpk(bf_lo(x0[0]) + s0[0] * silu_f(y0[0]), bf_hi(x0[0]) + s0[1] * silu_f(y0[1])),
                    cvtpk(bf_lo(x0[1]) + s0[2] * silu_f(y0[2]), bf_hi(x0[1]) + s0[3] * silu_f(y0[3]))};
  const u32x2 r1 = {cvtpk(bf_lo(x1[0]) + s1[0] * silu_f(y1[0]), bf_hi(x1[0]) + s1[1] * silu_f(y1[1])),
                    cvtpk(bf_lo(x1[1]) + s1[2] * silu_f(y1[2]), bf_hi(x1[1]) + s1[3] * silu_f(y1[3]))};
  *(u32x2*)(out + xo) = r0;
  *(u32x2*)(out + xo + 16 * D_) = r1;
}

extern "C" void kernel_launch(void* const* d_in, const int* in_sizes, int n_in, void* d_out, int out_size, void* d_ws,
                              size_t ws_size, hipStream_t stream) {
  const unsigned short* x = (const unsigned short*)d_in[0];
  const unsigned short* wqkv = (const unsigned short*)d_in[1];
  const unsigned short* wphi = (const unsigned short*)d_in[2];
  const unsigned short* wo = (const unsigned short*)d_in[3];
  const unsigned short* wg = (const unsigned short*)d_in[5];
  const unsigned short* wu = (const unsigned short*)d_in[6];
  const unsigned short* wd = (const unsigned short*)d_in[7];
  char* ws = (char*)d_ws;
  k_prep<<<256, 512, 0, stream>>>(x, wqkv, wo, wphi, (const unsigned*)wg, (const unsigned*)wu, (const unsigned*)wd, ws);
  k_main<<<256, 512, 0, stream>>>(wg, wu, wd, ws);
  k_final<<<256, 576, 0, stream>>>(x, ws, (unsigned short*)d_out);
}
```

```cpp
#include <hip/hip_runtime.h>
#include <hip/hip_bf16.h>
#include <stdint.h>

typedef __attribute__((ext_vector_type(8))) short s16x8;
typedef __attribute__((ext_vector_type(4))) short s16x4;
typedef __attribute__((ext_vector_type(8))) __bf16 bf16x8;
typedef __attribute__((ext_vector_type(2))) __bf16 bf16x2;
typedef __attribute__((ext_vector_type(16))) float f32x16;
typedef __attribute__((ext_vector_type(4))) float f32x4;
typedef __attribute__((ext_vector_type(2))) float f32x2;
typedef __attribute__((ext_vector_type(4))) unsigned u32x4;
typedef __attribute__((ext_vector_type(2))) unsigned u32x2;
typedef __attribute__((ext_vector_type(4))) int i32x4;
#define LAS __attribute__((address_space(3)))
#define GAS __attribute__((address_space(1)))
#define DEV __device__ __forceinline__

template <int I> struct IC { static constexpr int v = I; };
template <int B, int E, class Fn> DEV void sfor(Fn&& fn) {
  if constexpr (B < E) { fn(IC<B>{}); sfor<B + 1, E>(fn); }
}

constexpr int S_ = 2048, D_ = 576, F_ = 4608;
constexpr size_t WS_XN = 0;
constexpr size_t WS_WOT = WS_XN + 64 * 36 * 1024;
constexpr size_t WS_KVP = WS_WOT + 36 * 18 * 1024;
constexpr size_t WS_O = WS_KVP + 32 * 16 * 3840;
constexpr size_t WS_CNT = WS_O + 2048 * 576 * 2;
constexpr size_t WS_KVF = WS_CNT + 32 * 256;
constexpr size_t WS_QI = WS_KVF + 16 * 4096;
constexpr size_t WS_YP = WS_QI + 16 * 6 * 24576;
constexpr size_t YP_BYTES = (size_t)16 * 64 * 36 * 1024;

DEV unsigned cvtpk(float lo, float hi) {
  f32x2 v = {lo, hi};
  return __builtin_bit_cast(unsigned, __builtin_convertvector(v, bf16x2));
}
DEV float bf_lo(unsigned u) { return __uint_as_float(u << 16); }
DEV float bf_hi(unsigned u) { return __uint_as_float(u & 0xffff0000u); }
DEV f32x16 mfma32(u32x4 a, u32x4 b, f32x16 c) {
  return __builtin_amdgcn_mfma_f32_32x32x16_bf16(__builtin_bit_cast(bf16x8, a), __builtin_bit_cast(bf16x8, b), c, 0, 0, 0);
}
DEV f32x4 mfma16(u32x4 a, u32x4 b, f32x4 c) {
  return __builtin_amdgcn_mfma_f32_16x16x32_bf16(__builtin_bit_cast(bf16x8, a), __builtin_bit_cast(bf16x8, b), c, 0, 0, 0);
}
DEV u32x2 trd(unsigned addr) {
  return __builtin_bit_cast(u32x2, __builtin_amdgcn_ds_read_tr16_b64_v4i16((LAS s16x4*)(uintptr_t)addr));
}
DEV u32x4 trfrag(unsigned a0, unsigned a1) {
  u32x2 lo = trd(a0), hi = trd(a1);
  u32x4 r = {lo[0], lo[1], hi[0], hi[1]};
  return r;
}
DEV u32x4 lds_rd128(unsigned a) { return *(LAS u32x4*)(uintptr_t)a; }
DEV u32x2 lds_rd64(unsigned a) { return *(LAS u32x2*)(uintptr_t)a; }
DEV void lds_wr128(unsigned a, u32x4 v) { *(LAS u32x4*)(uintptr_t)a = v; }
DEV void lds_wr64(unsigned a, u32x2 v) { *(LAS u32x2*)(uintptr_t)a = v; }
DEV i32x4 mkdesc(const void* p, unsigned bytes) {
  uint64_t a = (uint64_t)p;
  i32x4 d = {(int)__builtin_amdgcn_readfirstlane((unsigned)a), (int)__builtin_amdgcn_readfirstlane((unsigned)(a >> 32) & 0xffffu),
             (int)bytes, 0x00020000};
  return d;
}
DEV void dma16(unsigned lds, unsigned voff, i32x4 desc, unsigned soff) {
  asm volatile("s_mov_b32 m0, %0\n\ts_nop 0\n\tbuffer_load_dwordx4 %1, %2, %3 offen lds"
               :: "s"(lds), "v"(voff), "s"(desc), "s"(soff) : "memory");
}
template <unsigned OFF> DEV void dma16i(unsigned lds, unsigned voff, i32x4 desc, unsigned soff) {
  asm volatile("s_add_u32 m0, %0, %4\n\ts_nop 0\n\tbuffer_load_dwordx4 %1, %2, %3 offen lds"
               :: "s"(lds), "v"(voff), "s"(desc), "s"(soff), "i"(OFF) : "memory", "scc");
}
#define WAITV(n) asm volatile("s_waitcnt vmcnt(" #n ")" ::: "memory")
#define SCHED() __builtin_amdgcn_sched_barrier(0)
DEV unsigned opq(unsigned v) { asm volatile("" : "+v"(v)); return v; }
#define WAITL0() asm volatile("s_waitcnt lgkmcnt(0)" ::: "memory")
#define BAR() do { asm volatile("" ::: "memory"); __builtin_amdgcn_s_barrier(); asm volatile("" ::: "memory"); } while (0)
DEV float xhalf_max(float x) {
  auto t = __builtin_amdgcn_permlane32_swap(__float_as_uint(x), __float_as_uint(x), false, false);
  return fmaxf(__uint_as_float(t[0]), __uint_as_float(t[1]));
}
DEV float xhalf_sum(float x) {
  auto t = __builtin_amdgcn_permlane32_swap(__float_as_uint(x), __float_as_uint(x), false, false);
  return __uint_as_float(t[0]) + __uint_as_float(t[1]);
}
constexpr float LOG2E = 1.4426950408889634f;
DEV float silu_f(float g) { return g * __builtin_amdgcn_rcpf(1.0f + __builtin_amdgcn_exp2f(-LOG2E * g)); }

DEV void hedgehog(const float (&pv)[10], int h, u32x4& pos, u32x4& neg, u32x2& ext) {
  float mx = pv[0], mn = pv[0];
#pragma unroll
  for (int r = 1; r < 8; ++r) { mx = fmaxf(mx, pv[r]); mn = fminf(mn, pv[r]); }
  if (h == 0) { mx = fmaxf(mx, fmaxf(pv[8], pv[9])); mn = fminf(mn, fminf(pv[8], pv[9])); }
  mx = xhalf_max(mx); mn = -xhalf_max(-mn);
  float ep[10], en[10], sp = 0.f, sn = 0.f;
#pragma unroll
  for (int r = 0; r < 10; ++r) {
    ep[r] = __builtin_amdgcn_exp2f((pv[r] - mx) * LOG2E);
    en[r] = __builtin_amdgcn_exp2f((mn - pv[r]) * LOG2E);
    if (r >= 8) { ep[r] = h ? 0.f : ep[r]; en[r] = h ? 0.f : en[r]; }
    sp += ep[r]; sn += en[r];
  }
  float ip = __builtin_amdgcn_rcpf(xhalf_sum(sp)), in = __builtin_amdgcn_rcpf(xhalf_sum(sn));
#pragma unroll
  for (int i = 0; i < 4; ++i) {
    pos[i] = cvtpk(ep[2 * i] * ip, ep[2 * i + 1] * ip);
    neg[i] = cvtpk(en[2 * i] * in, en[2 * i + 1] * in);
  }
  ext[0] = cvtpk(ep[8] * ip, ep[9] * ip);
  ext[1] = cvtpk(en[8] * in, en[9] * in);
}

__global__ __launch_bounds__(512) void k_prep(const unsigned short* __restrict__ x, const unsigned short* __restrict__ wqkv,
                                              const unsigned short* __restrict__ wo,
                                              const unsigned short* __restrict__ wphi, char* __restrict__ ws) {
  const int b = blockIdx.x, w = threadIdx.x >> 6, l = threadIdx.x & 63;
  if (w < 2) {
    const int tok = 8 * b + 4 * w + (l >> 4), i = l & 15;
    u32x2 v[9];
#pragma unroll
    for (int u = 0; u < 9; ++u) v[u] = *(const u32x2*)(x + (size_t)tok * D_ + 4 * (i + 16 * u));
    float f[36], sum = 0.f;
#pragma unroll
    for (int u = 0; u < 9; ++u) {
      f[4 * u] = bf_lo(v[u][0]); f[4 * u + 1] = bf_hi(v[u][0]); f[4 * u + 2] = bf_lo(v[u][1]); f[4 * u + 3] = bf_hi(v[u][1]);
      sum += (f[4 * u] + f[4 * u + 1]) + (f[4 * u + 2] + f[4 * u + 3]);
    }
#pragma unroll
    for (int m = 1; m < 16; m <<= 1) sum += __shfl_xor(sum, m);
    const float mean = sum * (1.0f / D_);
    float sq = 0.f;
#pragma unroll
    for (int e = 0; e < 36; ++e) { f[e] -= mean; sq += f[e] * f[e]; }
#pragma unroll
    for (int m = 1; m < 16; m <<= 1) sq += __shfl_xor(sq, m);
    const float rs = 1.0f / sqrtf(sq * (1.0f / D_) + 1e-5f);
    char* dst = ws + WS_XN + (size_t)(tok >> 5) * 36864 + (tok & 31) * 16;
#pragma unroll
    for (int u = 0; u < 9; ++u) {
      const int c8 = i + 16 * u, c = c8 >> 1;
      u32x2 o = {cvtpk(f[4 * u] * rs, f[4 * u + 1] * rs), cvtpk(f[4 * u + 2] * rs, f[4 * u + 3] * rs)};
      *(u32x2*)(dst + (c >> 1) * 1024 + (c & 1) * 512 + (c8 & 1) * 8) = o;
    }
  } else if (w >= 5) {
    u32x4 v[3];
#pragma unroll
    for (int m = 0; m < 3; ++m) {
      const int id = 576 * b + (int)threadIdx.x - 320 + 192 * m, jj = id / 9216, g = (id % 9216) / 1536;
      const int c = min(id % 1536, 1439), k16 = c / 240, r = c % 240, seg = r / 80, pos = (r % 80) / 5, ch = r % 5;
      const int row = 96 * g + 16 * k16 + 8 * ((pos >> 1) & 1) + 4 * (pos & 1) + (pos >> 2);
      const int off = min(row * 3456 + seg * 1152 + ((72 * jj) & ~15) + 16 * ch, D_ * 3 * D_ * 2 - 16);
      v[m] = *(const u32x4*)((const char*)wqkv + off);
    }
#pragma unroll
    for (int m = 0; m < 3; ++m) {
      const int id = 576 * b + (int)threadIdx.x - 320 + 192 * m, g = (id % 9216) / 1536;
      if (!(id % 1536 >= 1440 && (g == 2 || g == 3))) *(u32x4*)(ws + WS_QI + (size_t)id * 16) = v[m];
    }
  } else {
    for (int fi = 3 * b + (w - 2); fi < 648 + 64; fi += 768) {
      unsigned short e[8];
      char* dst;
      if (fi < 648) {
        const int hb = fi / 18, s = fi % 18, i = l & 15, g = l >> 4, gi = i >> 2;
        const int d = 16 * hb + 8 * (gi & 1) + 4 * (gi >> 1) + (i & 3);
#pragma unroll
        for (int jj = 0; jj < 8; ++jj) e[jj] = wo[(size_t)(32 * s + 8 * g + jj) * D_ + d];
        dst = ws + WS_WOT + (size_t)fi * 1024;
      } else {
        const int q = fi - 648, head = q >> 1, s = q & 1, f = l & 31, h = l >> 5;
#pragma unroll
        for (int jj = 0; jj < 8; ++jj) {
          const int d = 16 * s + 8 * (jj >> 2) + 4 * h + (jj & 3) - 2 * (head & 1);
          const bool ok = f < 18 && d >= 0 && d < 18;
          const unsigned short t = wphi[head * 324 + (ok ? d * 18 + f : 0)];
          e[jj] = ok ? t : (unsigned short)0;
        }
        dst = ws + WS_QI + (size_t)((head >> 1) * 6 + 2 + (head & 1)) * 24576 + 23040;
        if (s) {
          *(u32x2*)(dst + 1024 + l * 8) = u32x2{e[0] | ((unsigned)e[1] << 16), e[2] | ((unsigned)e[3] << 16)};
          continue;
        }
      }
      u32x4 o = {e[0] | ((unsigned)e[1] << 16), e[2] | ((unsigned)e[3] << 16), e[4] | ((unsigned)e[5] << 16), e[6] | ((unsigned)e[7] << 16)};
      *(u32x4*)(dst + l * 16) = o;
    }
  }
  if (b == 0 && threadIdx.x < 32) *(unsigned*)(ws + WS_CNT + threadIdx.x * 256) = 0u;
}

constexpr unsigned SLOT = 24576, HALF = 3 * SLOT, SPARE = 6 * SLOT, LDS_MAIN = 163840;
constexpr int RY = 1, RO = 8;
constexpr int NA = 3, NB = 6, NQ = 6;
constexpr int YAUX = 16, YLD = 2;
constexpr int YH = 0;

__global__ __launch_bounds__(512, 2) void k_main(const unsigned short* __restrict__ wg, const unsigned short* __restrict__ wu,
                                                 const unsigned short* __restrict__ wd, char* __restrict__ ws) {
  __shared__ __attribute__((aligned(1024))) char lds[LDS_MAIN];
  const unsigned L0 = (unsigned)(uintptr_t)(LAS char*)lds;
  const int b = blockIdx.x, tid = threadIdx.x;
  const int j = 4 * (b & 3) + ((b >> 3) & 3), t = ((b >> 2) & 1) * 8 + (b >> 5), jo = j & 1;
  const int w = __builtin_amdgcn_readfirstlane(tid >> 6), tg4 = w >> 1, p = w & 1;
  int l = tid & 63, h = l >> 5, g16 = (l >> 4) & 1, q4 = (l >> 2) & 3, pp = l & 3;
  const int tg = 4 * t + tg4;
  GAS unsigned* const cnt1 = (GAS unsigned*)(ws + WS_CNT + j * 256);
  GAS unsigned* const cnt2 = cnt1 + 1024;

  const i32x4 dW = mkdesc(ws, (unsigned)WS_YP), dA = mkdesc(p ? wu : wg, D_ * F_ * 2), dB = mkdesc(wd, D_ * F_ * 2);
  unsigned l16 = l * 16;
  const unsigned vA2 = (l >> 3) * 9216 + ((((l >> 1) & 3) ^ (((l >> 4) & 1) + 2 * p)) * 32) + (l & 1) * 16;
  unsigned vA1 = 0, vB = 0;
  auto pieceX = [&](int half, int m) {
    const int n = 9 * w + m, tq = w >> 1, s = 18 * half + n - 18 * tq;
    dma16(L0 + n * 1024, l16, dW, (unsigned)WS_XN + ((4 * t + tq) * 36 + s) * 1024);
  };
  auto pieceQ = [&](int g, int m) {
    dma16(L0 + (3 + g % 3) * SLOT + (w + 8 * m) * 1024, l16, dW, (unsigned)WS_QI + (j * 6 + g) * SLOT + (w + 8 * m) * 1024);
  };
  auto pieceA2 = [&](int v, int i, int m) {
    dma16(L0 + i * SLOT + (w + 8 * m) * 1024, vA2, dA,
          (96 * i + 16 * ((w >> 2) + 2 * m) + 8 * (tg4 & 1)) * 9216 + (288 * j + 32 * jo + 64 * v) * 2);
  };
  auto pieceA1 = [&](int i, int m) {
    dma16(L0 + i * SLOT + (w + 8 * m) * 1024, vA1, dA, (192 * i + 16 * (tg4 + 4 * m)) * 9216 + (288 * j + 256 * (1 - jo)) * 2);
  };
  const bool grp = w >= 4;
  const unsigned wl = L0 + w * 1024;
  const unsigned sA2 = (16 * (w >> 2) + 8 * (tg4 & 1)) * 9216 + (288 * j + 32 * jo) * 2;
  unsigned sB[9];
#pragma unroll
  for (int e = 0; e < 9; ++e) {
    const int it = 12 * (e / 3) + tg4 + 4 * (e % 3), dp = it >= 18;
    sB[e] = (288 * j + 16 * (it - 18 * dp)) * 1152 + 128 * dp;
  }
  bool timeout = false;
  auto spin16 = [&](GAS unsigned* c, unsigned seen) {
    unsigned spins = 0;
    while (__builtin_amdgcn_readfirstlane(seen) < 16u) {
      __builtin_amdgcn_s_sleep(2);
      seen = __hip_atomic_load(c, __ATOMIC_RELAXED, __HIP_MEMORY_SCOPE_AGENT);
      if (++spins > (1u << 17)) { timeout = true; break; }
    }
    asm volatile("" ::: "memory");
  };

  u32x4 X[36], wp0, wp1;
#pragma unroll
  for (int m = 0; m < 9; ++m) pieceX(0, m);
#pragma unroll
  for (int m = 0; m < 9; ++m) pieceQ(m / 3, m % 3);
  WAITV(6); BAR();
  const unsigned rx = opq(L0 + tg4 * 18432 + l16);
#pragma unroll
  for (int s = 0; s < 18; ++s) X[s] = lds_rd128(rx + s * 1024);
  WAITL0(); BAR();
  {
    uint64_t pc;
    asm volatile("s_getpc_b64 %0" : "=s"(pc));
    asm volatile("s_mov_b32 m0, %0\n\ts_nop 0\n\tglobal_load_lds_dword %1, %2"
                 :: "s"(L0 + SPARE + w * 256), "v"(min(tid, 271) * 128), "s"(pc) : "memory");
  }

  f32x16 aq = {}, ak = {}, av = {};
  {
    const unsigned rq = L0 + 3 * SLOT + (4 * q4 + 2 * h) * 80 + ((72 * j) & 15) + 32 * p + 32 * g16 + 8 * pp;
#define QADDR(n) (rb[((n) / 18) & 1] + (((n) % 18) / 3) * 3840 + ((n) % 3) * 1280)
    u32x4 a[NQ];
    unsigned rb[2] = {rq, rq};
    sfor<0, 108>([&](auto nn) {
      constexpr int n = decltype(nn)::v, g = n / 18, r = n % 18;
      if (r == 0) {
        if (g == 1 || g == 4) WAITV(9);
        if (g == 3) WAITV(3);
        if (g) BAR();
        rb[(g + 1) & 1] = opq(rq + ((g + 1) % 3) * SLOT);
        if (g == 3) {
#pragma unroll
          for (int s = 18; s < 36; ++s) X[s] = lds_rd128(rx + (s - 18) * 1024);
          const unsigned wa = L0 + (p ? 3 : 5) * SLOT + 23040;
          wp0 = lds_rd128(wa + l16);
          const u32x2 w1 = lds_rd64(wa + 1024 + l * 8);
          wp1 = u32x4{w1[0], w1[1], 0u, 0u};
        }
        if (g == 0 || g == 3) {
#pragma unroll
          for (int e = 0; e < NQ; ++e) a[e] = trfrag(QADDR(n + e), QADDR(n + e) + 80);
        }
        if (g == 3) { WAITL0(); BAR(); }
        SCHED();
      }
      if (g == 0 && r < 9) pieceX(1, r);
      if (n == 10) { WAITV(13); BAR(); }
      if ((g == 1 || g == 2) && r < 3) pieceQ(g + 2, r);
      if (g == 3 && r < 3) pieceQ(5, r);
      if (g == 3 && r >= 3 && r < 12) pieceA2(0, (r - 3) / 3, r % 3);
      if (g >= 4 && r % 6 == 1) pieceA2(0, g - 1, r / 6);
      constexpr int s = n / 3;
      if (n % 3 == 0) aq = mfma32(a[n % NQ], X[s], aq);
      else if (n % 3 == 1) ak = mfma32(a[n % NQ], X[s], ak);
      else av = mfma32(X[s], a[n % NQ], av);
      if (n + NQ < 108 && (n + NQ < 54 || n >= 54)) a[n % NQ] = trfrag(QADDR(n + NQ), QADDR(n + NQ) + 80);
      if (n == 107 || n == 53) asm volatile("" : "+v"(aq), "+v"(ak), "+v"(av));
      SCHED();
    });
  }
  constexpr unsigned PQ_OFF = 5 * SLOT + 8 * 3840;
  if (h) {
    const unsigned dst = L0 + PQ_OFF + 4 * 2048 + w * 256;
#pragma unroll
    for (int e = 0; e < 2; ++e) {
      const int id = t * 180 + tg4 * 45 + min(l - 32 + 32 * e, 44);
      asm volatile("s_mov_b32 m0, %0\n\ts_nop 0\n\tbuffer_load_dword %1, %2, %3 offen lds"
                   :: "s"(dst), "v"((id / 5) * 9216 + (id % 5) * 128), "s"(dA), "s"((576 * j) & ~127) : "memory");
    }
    asm volatile("s_mov_b32 m0, %0\n\ts_nop 0\n\tbuffer_load_dword %1, %2, %3 offen lds"
                 :: "s"(dst), "v"((t * 162 + min(w * 21 + min(l - 32, 20), 161)) * 128), "s"(dB), "s"(288 * j * 1152) : "memory");
  }
  {
    auto phi = [&](const f32x16& c) {
      u32x4 b0 = {cvtpk(c[0], c[1]), cvtpk(c[2], c[3]), cvtpk(c[4], c[5]), cvtpk(c[6], c[7])};
      u32x4 b1 = {cvtpk(c[8], c[9]), cvtpk(c[10], c[11]), 0u, 0u};
      b0[0] = (p && !h) ? 0u : b0[0];
      b1[0] = h ? 0u : b1[0];
      b1[1] = (p && !h) ? b1[1] : 0u;
      f32x16 z = {};
      z = mfma32(wp0, b0, z);
      return mfma32(wp1, b1, z);
    };
    const f32x16 pq = phi(aq), pk = phi(ak);
#pragma unroll
    for (int i = 0; i < 5; ++i) *(LAS unsigned*)(uintptr_t)(L0 + PQ_OFF + i * 2048 + tid * 4) = cvtpk(pq[2 * i], pq[2 * i + 1]);
    float pv[10];
#pragma unroll
    for (int i = 0; i < 5; ++i) { const unsigned u = cvtpk(pk[2 * i], pk[2 * i + 1]); pv[2 * i] = bf_lo(u); pv[2 * i + 1] = bf_hi(u); }
    u32x4 pos, neg; u32x2 ext;
    hedgehog(pv, h, pos, neg, ext);
    BAR();
    const unsigned R = L0 + 5 * SLOT + w * 3840, tk = l & 31;
    lds_wr64(R + tk * 64 + 8 * h, u32x2{pos[0], pos[1]});
    lds_wr64(R + tk * 64 + 16 + 8 * h, u32x2{pos[2], pos[3]});
    lds_wr64(R + tk * 64 + 32 + 8 * h, u32x2{neg[0], neg[1]});
    lds_wr64(R + tk * 64 + 48 + 8 * h, u32x2{neg[2], neg[3]});
    lds_wr64(R + 2048 + l * 8, ext);
    asm volatile("" ::: "memory");
    f32x16 kv0 = {}, kv1 = {};
#pragma unroll
    for (int s = 0; s < 2; ++s) {
      u32x4 vb;
#pragma unroll
      for (int i = 0; i < 4; ++i) vb[i] = (tk == 20) ? 0x3f803f80u : cvtpk(av[8 * s + 2 * i], av[8 * s + 2 * i + 1]);
      const unsigned r0 = 16 * s + 4 * h + q4;
      kv0 = mfma32(trfrag(R + r0 * 64 + 32 * g16 + 8 * pp, R + (r0 + 8) * 64 + 32 * g16 + 8 * pp), vb, kv0);
      kv1 = mfma32(trfrag(R + 2048 + r0 * 8, R + 2048 + (r0 + 8) * 8), vb, kv1);
    }
    asm volatile("" ::: "memory");
    if (tk < 24) {
      const unsigned Rl = R + (h * 24 + tk) * 16;
#pragma unroll
      for (int qd = 0; qd < 4; ++qd)
        lds_wr128(Rl + qd * 768, __builtin_bit_cast(u32x4, f32x4{kv0[4 * qd], kv0[4 * qd + 1], kv0[4 * qd + 2], kv0[4 * qd + 3]}));
      lds_wr128(Rl + 3072, __builtin_bit_cast(u32x4, f32x4{kv1[0], kv1[1], kv1[2], kv1[3]}));
    }
    WAITL0(); WAITV(9); BAR();
    if (tid >= 272) {
      const int rem = tid - 272;
#pragma unroll
      for (int pq_ = 0; pq_ < 2; ++pq_) {
        f32x4 sum = {};
#pragma unroll
        for (int e = 0; e < 4; ++e) sum += __builtin_bit_cast(f32x4, lds_rd128(L0 + 5 * SLOT + (2 * e + pq_) * 3840 + rem * 16));
        __builtin_amdgcn_raw_buffer_store_b128(__builtin_bit_cast(u32x4, sum),
            __builtin_amdgcn_make_buffer_rsrc(ws + WS_KVP, 0, 32 * 16 * 3840, 0x00020000),
            ((2 * j + pq_) * 16 + t) * 3840 + rem * 16, 0, 16);
      }
    }
    WAITL0();
  }

  u32x4 H[18], k0, k1, k2;
  {
    u32x4 a[NA], hh[9];
    auto act = [&](const f32x16& c) {
      u32x4 r;
#pragma unroll
      for (int i = 0; i < 4; ++i) r[i] = cvtpk(silu_f(c[2 * i]) * c[8 + 2 * i], silu_f(c[2 * i + 1]) * c[9 + 2 * i]);
      return r;
    };
    const unsigned rA2 = L0 + h * 2048 + g16 * 1024 + q4 * 128 + (((2 * p) ^ ((q4 >> 1) + 2 * g16)) * 32) + pp * 8;
    const auto kvp = __builtin_amdgcn_make_buffer_rsrc(ws + WS_KVP, 0, 32 * 16 * 3840, 0x00020000);
    const auto kvf = __builtin_amdgcn_make_buffer_rsrc(ws + WS_KVF + j * 4096, 0, 4096, 0x00020000);
#pragma unroll
    for (int e = 0; e < NA; ++e) { const unsigned ad = (rA2 ^ ((e & 1) * 32)) + (e >> 1) * 4096; a[e] = trfrag(ad, ad + 512); }
    f32x16 c0 = {}, c1 = {};
    unsigned pk[16];
    float e0, e1, u0, u1;
    auto opf = [](float x) { asm volatile("" : "+v"(x)); return x; };
    auto micro = [&](auto nn, auto vv) {
      constexpr int n = decltype(nn)::v, d = n / 8, k = n % 8, gi = (d / 4) * 8 + d % 4, v = decltype(vv)::v;
      if constexpr (n >= 0 && n < 64) {
        if (k == 0) e0 = opf(-LOG2E * bf_lo(pk[gi]));
        if (k == 1) { e0 = opf(__builtin_amdgcn_exp2f(e0)); e1 = opf(bf_hi(pk[gi])); }
        if (k == 2) { e0 = opf(1.0f + e0); u1 = opf(-LOG2E * e1); }
        if (k == 3) { u1 = opf(__builtin_amdgcn_exp2f(u1)); u0 = opf(bf_hi(pk[gi + 4])); }
        if (k == 4) { e0 = opf(__builtin_amdgcn_rcpf(e0)); u1 = opf(1.0f + u1); }
        if (k == 5) { u1 = opf(__builtin_amdgcn_rcpf(u1)); e1 = opf(e1 * u0); }
        if (k == 6) u0 = opf(bf_lo(pk[gi]) * bf_lo(pk[gi + 4]));
        if (k == 7) hh[2 * v + d / 4][d % 4] = opq(cvtpk(u0 * e0, e1 * u1));
      }
    };
    auto dunit = [&](auto vv) {
      constexpr int v = decltype(vv)::v;
      constexpr bool last = v == 3;
      unsigned rb[2][2], seen = 0;
      u32x4 kl = {}, kl2 = {};
      const unsigned sv = sA2 + 128 * v;
      rb[0][0] = opq(rA2); rb[0][1] = opq(rA2 ^ 32);
      sfor<0, 72>([&](auto nn) {
        constexpr int n = decltype(nn)::v, i = n / 12, r = n % 12;
        if (r == 0) {
          if (((i == 1 && v == 0) || (i == 0 && v == 2)) && grp) WAITV(0); else WAITV(9);
          BAR();
          if (!last) {
            if (i == 1 && v == 0 && tid == 0) __hip_atomic_fetch_add(cnt1, 1u, __ATOMIC_RELAXED, __HIP_MEMORY_SCOPE_AGENT);
            if (i == 0 && v == 2 && tid == 0) __hip_atomic_fetch_add(cnt2, 1u, __ATOMIC_RELAXED, __HIP_MEMORY_SCOPE_AGENT);
            if (v == 1 && grp) {
              if (i == 0) seen = __hip_atomic_load(cnt1, __ATOMIC_RELAXED, __HIP_MEMORY_SCOPE_AGENT);
              if (i == 2) {
                spin16(cnt1, seen);
                const int I = 30 * t + min((tid - 256) >> 3, 29), pq_ = I >= 240;
                const int o = ((2 * j + pq_) * 16 + (tid & 7)) * 3840 + (I - 240 * pq_) * 16;
                kl = __builtin_amdgcn_raw_buffer_load_b128(kvp, o, 0, 16);
                kl2 = __builtin_amdgcn_raw_buffer_load_b128(kvp, o, 8 * 3840, 16);
              }
              if (i == 4) {
                f32x4 sm = __builtin_bit_cast(f32x4, kl) + __builtin_bit_cast(f32x4, kl2);
#pragma unroll
                for (int e = 0; e < 4; ++e) {
                  sm[e] += __int_as_float(__builtin_amdgcn_update_dpp(0, __float_as_int(sm[e]), 0xB1, 0xf, 0xf, true));
                  sm[e] += __int_as_float(__builtin_amdgcn_update_dpp(0, __float_as_int(sm[e]), 0x4E, 0xf, 0xf, true));
                  sm[e] += __int_as_float(__builtin_amdgcn_update_dpp(0, __float_as_int(sm[e]), 0x141, 0xf, 0xf, true));
                }
                if (timeout) sm[0] = __uint_as_float(0x7fc00000u);
                if ((tid & 7) == 0 && tid < 496)
                  __builtin_amdgcn_raw_buffer_store_b64(u32x2{cvtpk(sm[0], sm[1]), cvtpk(sm[2], sm[3])}, kvf, (30 * t + ((tid - 256) >> 3)) * 8, 0, 16);
              }
            }
          }
          if (i < 5) { rb[(i + 1) & 1][0] = opq(rA2 + (i + 1) * SLOT); rb[(i + 1) & 1][1] = opq((rA2 ^ 32) + (i + 1) * SLOT); }
          else { rb[0][0] = opq(rA2); rb[0][1] = opq(rA2 ^ 32); }
          SCHED();
        }
        auto pieces = [&]() {
          sfor<0, 3>([&](auto mm) {
            constexpr int m = decltype(mm)::v;
            if (i == 0) dma16i<5 * SLOT + m * 8192>(wl, vA2, dA, sv + (480 + 32 * m) * 9216);
            else if (!last) dma16i<(i - 1) * SLOT + m * 8192>(wl, vA2, dA, sv + 128 + (96 * (i - 1) + 32 * m) * 9216);
            else if (i < 4) pieceA1(i - 1, m);
            else dma16i<(i - 1) * SLOT + m * 8192>(wl, vB, dB, sB[(i == 5) * 3 + m]);
          });
        };
        if (r == RY && grp) pieces();
        if (n < 2) { const f32x16 z = {}; if (n == 0) c0 = mfma32(a[0], X[0], z); else c1 = mfma32(a[1], X[0], z); }
        else if (n % 2 == 0) c0 = mfma32(a[n % NA], X[n / 2], c0);
        else c1 = mfma32(a[n % NA], X[n / 2], c1);
        constexpr int q = n + NA;
        const unsigned ad = rb[(q / 12) & 1][q & 1] + ((q % 12) >> 1) * 4096;
        if (!(last && q >= 72)) a[n % NA] = trfrag(ad, ad + 512);
        if constexpr (v > 0) micro(IC<n - 2>{}, IC<v - 1>{});
        if (r == RO && !grp) pieces();
        SCHED();
      });
#pragma unroll
      for (int e = 0; e < 8; ++e) { pk[e] = opq(cvtpk(c0[2 * e], c0[2 * e + 1])); pk[8 + e] = opq(cvtpk(c1[2 * e], c1[2 * e + 1])); }
      SCHED();
    };
    dunit(IC<0>{}); dunit(IC<1>{}); dunit(IC<2>{});
    l = opq(__builtin_amdgcn_mbcnt_hi(~0u, __builtin_amdgcn_mbcnt_lo(~0u, 0u)));
    vA1 = (l >> 2) * 9216 + ((((l >> 1) & 1) ^ p) * 32) + (l & 1) * 16;
    vB = (8 * p + 4 * (l >> 5) + ((l >> 2) & 3)) * 1152 + 64 * ((l >> 4) & 1) + 16 * (l & 3);
    dunit(IC<3>{});
    l = opq(__builtin_amdgcn_mbcnt_hi(~0u, __builtin_amdgcn_mbcnt_lo(~0u, 0u)));
    h = l >> 5; g16 = (l >> 4) & 1; q4 = (l >> 2) & 3; pp = l & 3; l16 = l * 16;
    const unsigned rA1 = L0 + g16 * 1024 + h * 512 + q4 * 64 + (p ^ g16) * 32 + pp * 8;
    u32x2 kc[5];
    {
      const unsigned r0 = opq(rA1), r2 = opq(rA1 + 2 * SLOT);
      unsigned seen = __hip_atomic_load(cnt2, __ATOMIC_RELAXED, __HIP_MEMORY_SCOPE_AGENT);
      c0 = f32x16{};
      sfor<0, 36>([&](auto nn) {
        constexpr int s = decltype(nn)::v;
        if (s == 0) {
#pragma unroll
          for (int e = 0; e < NA; ++e) a[e] = trfrag(r0 + e * 2048, r0 + e * 2048 + 256);
          WAITV(9); BAR();
        }
        if (s == 12) {
          WAITV(9); BAR();
          spin16(cnt2, seen);
          const unsigned ka = (p * 240 + h * 24 + min(l & 31, 23)) * 8;
#pragma unroll
          for (int e = 0; e < 5; ++e) kc[e] = __builtin_amdgcn_raw_buffer_load_b64(kvf, ka + e * 384, 0, 16);
          SCHED();
        }
        if ((s == RY && grp) || (s == RO && !grp))
          sfor<0, 3>([&](auto mm) { constexpr int m = decltype(mm)::v; dma16i<5 * SLOT + m * 8192>(wl, vB, dB, sB[6 + m]); });
        c0 = mfma32(a[s % NA], X[s], c0);
        constexpr int q = s + NA;
        if (q < 36) { const unsigned ad = q < 24 ? r0 + q * 2048 : r2 + (q - 24) * 2048; a[s % NA] = trfrag(ad, ad + 256); }
        micro(IC<2 * s - 4>{}, IC<3>{}); micro(IC<2 * s - 3>{}, IC<3>{});
        SCHED();
      });
      hh[8] = act(c0);
    }
    BAR();
#pragma unroll
    for (int e = 0; e < 8; ++e) lds_wr128(L0 + (tg4 * 18 + 2 * jo + 4 * (e >> 1) + 2 * p + (e & 1)) * 1024 + l16, hh[e]);
    lds_wr128(L0 + (tg4 * 18 + 16 * (1 - jo) + p) * 1024 + l16, hh[8]);
    WAITL0(); BAR();
#pragma unroll
    for (int s = 0; s < 18; ++s) H[s] = lds_rd128(L0 + tg4 * 18432 + l16 + s * 1024);
    k0 = u32x4{kc[0][0], kc[0][1], kc[1][0], kc[1][1]}; k1 = u32x4{kc[2][0], kc[2][1], kc[3][0], kc[3][1]};
    k2 = u32x4{h ? 0u : kc[4][0], h ? 0u : kc[4][1], 0u, 0u};
    WAITL0();
    WAITV(0); BAR();
  }
  {
    unsigned cur = L0 + HALF, oth = L0;
    const unsigned rB = h * 512 + p * 256 + q4 * 64 + g16 * 32 + pp * 8;
    const auto yr = __builtin_amdgcn_make_buffer_rsrc(ws + WS_YP, 0, (unsigned)YP_BYTES, 0x00020000);
    const unsigned ybase = ((tg * 36 + 2 * p) * 16 + j) * 1024;
    u32x4 a[NB];
#pragma unroll
    for (int e = 0; e < NB; ++e) a[e] = trfrag(cur + rB + e * 2048, cur + rB + e * 2048 + 1024);
    auto ocomp = [&]() {
    float pv[10];
#pragma unroll
    for (int i = 0; i < 5; ++i) {
      const unsigned u = *(LAS unsigned*)(uintptr_t)(L0 + PQ_OFF + i * 2048 + w * 256 + l * 4);
      pv[2 * i] = bf_lo(u); pv[2 * i + 1] = bf_hi(u);
    }
    u32x4 pos, neg; u32x2 ext;
    hedgehog(pv, h, pos, neg, ext);
    f32x16 on = {};
    on = mfma32(k0, pos, on); on = mfma32(k1, neg, on); on = mfma32(k2, u32x4{ext[0], ext[1], 0u, 0u}, on);
    const float inv = timeout ? __uint_as_float(0x7fc00000u) : __builtin_amdgcn_rcpf(xhalf_sum(h ? on[8] : 0.f) + 1e-6f);
    char* op = ws + WS_O + (size_t)(32 * tg + (l & 31)) * 1152 + (36 * j + 16 * p + 4 * h) * 2;
    const u32x2 o0 = {cvtpk(on[0] * inv, on[1] * inv), cvtpk(on[2] * inv, on[3] * inv)};
    const u32x2 o1 = {cvtpk(on[4] * inv, on[5] * inv), cvtpk(on[6] * inv, on[7] * inv)};
    const u32x2 o2 = {cvtpk(on[8] * inv, on[9] * inv), cvtpk(on[10] * inv, on[11] * inv)};
    if (p && !h) *(unsigned*)(op + 4) = o0[1]; else *(u32x2*)op = o0;
    *(u32x2*)(op + 16) = o1;
    if (!h) { if (p) *(u32x2*)(op + 32) = o2; else *(unsigned*)(op + 32) = o2[0]; }
      };
    auto period = [&](auto last_, int P) {
      constexpr bool last = decltype(last_)::v;
      const unsigned rc = opq(cur + rB), rc2 = opq(cur + rB + 2 * SLOT), rn = opq(oth + rB);
      const unsigned wc = cur + w * 1024, wo_ = oth + w * 1024, sp = 256 * P;
      f32x16 acc = {};
      sfor<0, (last ? 18 : 36)>([&](auto nn) {
        constexpr int n = decltype(nn)::v, i = n / 12, r = n % 12;
        if (r == 0) {
          if (last) { if (i == 0) { WAITV(6); BAR(); } }
          else if (P == 3) { if (i == 0) WAITV(13); else if (i == 1) WAITV(10); else WAITV(7); BAR(); }
          else if (i > 0 || P > 0) { WAITV(11); BAR(); }
          if (!last && i == 0 && P == (grp ? 1 : 2)) ocomp();
          SCHED();
        }
        if (!last && i == 0 && ((r == RY && grp) || (r == 5 && !grp)) && P == 0)
          sfor<0, 9>([&](auto mm) { constexpr int m = decltype(mm)::v; dma16i<(m / 3) * SLOT + (m % 3) * 8192>(wl, vB, dB, sB[m] + 256); });
        if (!last && ((r == RY && grp) || (r == RO && !grp)) && P < 3 && (i > 0 || P > 0))
          sfor<0, 3>([&](auto mm) {
            constexpr int m = decltype(mm)::v;
            if (i == 0) dma16i<2 * SLOT + m * 8192>(wo_, vB, dB, sB[6 + m] + 256 + sp);
            else dma16i<(i ? i - 1 : 0) * SLOT + m * 8192>(wc, vB, dB, sB[(i ? i - 1 : 0) * 3 + m] + 512 + sp);
          });
        if (n % 18 == 0) { const f32x16 z = {}; acc = mfma32(a[n % NB], H[0], z); }
        else acc = mfma32(a[n % NB], H[n % 18], acc);
        constexpr int q = n + NB;
        const unsigned ad = q < 24 ? rc + q * 2048 : q < 36 ? rc2 + (q - 24) * 2048 : rn + (q - 36) * 2048;
        if (!(last && q >= 18)) a[n % NB] = trfrag(ad, ad + 1024);
        SCHED();
        if (n % 18 == 17) {
          const unsigned so = ybase + (2 * P + n / 18) * 65536;
#pragma unroll
          for (int e = 0; e < 2; ++e) {
            const auto s0 = __builtin_amdgcn_permlane16_swap(cvtpk(acc[8 * e], acc[8 * e + 1]), cvtpk(acc[8 * e + 4], acc[8 * e + 5]), false, false);
            const auto s1 = __builtin_amdgcn_permlane16_swap(cvtpk(acc[8 * e + 2], acc[8 * e + 3]), cvtpk(acc[8 * e + 6], acc[8 * e + 7]), false, false);
            const u32x4 y = {s0[0], s1[0], s0[1], s1[1]};
            __builtin_amdgcn_raw_buffer_store_b128(y, yr, l16, so + e * 16384, YAUX);
          }
          SCHED();
        }
      });
      const unsigned tmp = cur; cur = oth; oth = tmp;
    };
#pragma unroll 1
    for (int P = 0; P < 4; ++P) period(IC<0>{}, P);
    period(IC<1>{}, 4);
  }
}

__global__ __launch_bounds__(576, 3) void k_final(const unsigned short* __restrict__ x, const char* __restrict__ ws,
                                                  unsigned short* __restrict__ out) {
  __shared__ __attribute__((aligned(1024))) char lds[36864];
  const unsigned L0 = (unsigned)(uintptr_t)(LAS char*)lds;
  const int b = blockIdx.x, tg = (b & 7) * 8 + (b >> 5), dq = (b >> 3) & 3;
  const int w = __builtin_amdgcn_readfirstlane(threadIdx.x >> 6), l = threadIdx.x & 63, g = l >> 4, hb = 9 * dq + w;
  const i32x4 dO = mkdesc(ws + WS_O, S_ * D_ * 2);
  u32x4 yp[16], wf[18];
  const auto yr = __builtin_amdgcn_make_buffer_rsrc((void*)(ws + WS_YP), 0, (unsigned)YP_BYTES, 0x00020000);
  const auto wr = __builtin_amdgcn_make_buffer_rsrc((void*)(ws + WS_WOT), 0, 36 * 18 * 1024, 0x00020000);
#pragma unroll
  for (int jj = 0; jj < YH; ++jj) yp[jj] = __builtin_amdgcn_raw_buffer_load_b128(yr, l * 16, ((tg * 36 + hb) * 16 + jj) * 1024, YLD);
  SCHED();
#pragma unroll
  for (int m = 0; m < 4; ++m) {
    const int fi = 4 * w + m, tb = fi >= 18, s = fi - 18 * tb;
    dma16(L0 + fi * 1024, (l & 15) * 1152 + g * 16, dO, (32 * tg + 16 * tb) * 1152 + 64 * s);
  }
  SCHED();
#pragma unroll
  for (int s = 0; s < 18; ++s) wf[s] = __builtin_amdgcn_raw_buffer_load_b128(wr, l * 16, (hb * 18 + s) * 1024, 0);
  SCHED();
  const size_t xo = (size_t)(32 * tg + (l & 15)) * D_ + 16 * hb + 8 * (g & 1) + 4 * (g >> 1);
  const u32x2 x0 = *(const u32x2*)(x + xo), x1 = *(const u32x2*)(x + xo + 16 * D_);
#pragma unroll
  for (int jj = YH; jj < 16; ++jj) yp[jj] = __builtin_amdgcn_raw_buffer_load_b128(yr, l * 16, ((tg * 36 + hb) * 16 + jj) * 1024, YLD);
  SCHED();
  asm volatile("s_waitcnt vmcnt(%0)" :: "i"(36 - YH) : "memory");
  BAR();
  f32x4 s0 = {}, s1 = {};
#pragma unroll
  for (int s = 0; s < 18; ++s) {
    s0 = mfma16(wf[s], lds_rd128(L0 + s * 1024 + l * 16), s0);
    s1 = mfma16(wf[s], lds_rd128(L0 + (18 + s) * 1024 + l * 16), s1);
  }
  f32x4 y0 = {}, y1 = {};
#pragma unroll
  for (int jj = 0; jj < 16; ++jj) {
    y0 += f32x4{bf_lo(yp[jj][0]), bf_hi(yp[jj][0]), bf_lo(yp[jj][1]), bf_hi(yp[jj][1])};
    y1 += f32x4{bf_lo(yp[jj][2]), bf_hi(yp[jj][2]), bf_lo(yp[jj][3]), bf_hi(yp[jj][3])};
  }
  const u32x2 r0 = {cvtpk(bf_lo(x0[0]) + s0[0] * silu_f(y0[0]), bf_hi(x0[0]) + s0[1] * silu_f(y0[1])),
                    cvtpk(bf_lo(x0[1]) + s0[2] * silu_f(y0[2]), bf_hi(x0[1]) + s0[3] * silu_f(y0[3]))};
  const u32x2 r1 = {cvtpk(bf_lo(x1[0]) + s1[0] * silu_f(y1[0]), bf_hi(x1[0]) + s1[1] * silu_f(y1[1])),
                    cvtpk(bf_lo(x1[1]) + s1[2] * silu_f(y1[2]), bf_hi(x1[1]) + s1[3] * silu_f(y1[3]))};
  *(u32x2*)(out + xo) = r0;
  *(u32x2*)(out + xo + 16 * D_) = r1;
}

extern "C" void kernel_launch(void* const* d_in, const int* in_sizes, int n_in, void* d_out, int out_size, void* d_ws,
                              size_t ws_size, hipStream_t stream) {
  const unsigned short* x = (const unsigned short*)d_in[0];
  const unsigned short* wqkv = (const unsigned short*)d_in[1];
  const unsigned short* wphi = (const unsigned short*)d_in[2];
  const unsigned short* wo = (const unsigned short*)d_in[3];
  const unsigned short* wg = (const unsigned short*)d_in[5];
  const unsigned short* wu = (const unsigned short*)d_in[6];
  const unsigned short* wd = (const unsigned short*)d_in[7];
  char* ws = (char*)d_ws;
  k_prep<<<256, 512, 0, stream>>>(x, wqkv, wo, wphi, ws);
  k_main<<<256, 512, 0, stream>>>(wg, wu, wd, ws);
  k_final<<<256, 576, 0, stream>>>(x, ws, (unsigned short*)d_out);
}
```
